```python
import torch

def _softmax(x, axis=-1):
    return torch.softmax(x, dim=axis)
def _gelu(x):
    return torch.nn.functional.gelu(x, approximate="tanh")
def _silu(x):
    return torch.nn.functional.silu(x)
def _sigmoid(x):
    return torch.sigmoid(x)
def _layernorm(x, axis=-1, eps=1e-5):
    m = x.mean(dim=axis, keepdim=True)
    v = x.var(dim=axis, keepdim=True, unbiased=False)
    return (x - m) / torch.sqrt(v + eps)
def _scatter_add(tgt, idx, src, axis=0):
    return tgt.clone().index_add_(axis, idx, src)
def _segment_sum(src, seg, n):
    out = torch.zeros((n,) + src.shape[1:], dtype=src.dtype)
    return out.index_add_(0, seg, src)
def _pad(x, pw):
    flat = tuple(v for pair in reversed(pw) for v in pair)
    return torch.nn.functional.pad(x, flat)

kernel_name = 'concat_heads__compressed_kv_cla__linear_kernel_hedgehog__prompt'

def setup_inputs():

    _w = (lambda a: (lambda t: t.to(torch.bfloat16) if t.is_floating_point() else t)(torch.as_tensor(a)))
    def _A_setup():
        B, H, Sq, Sk, D, M = 1, 32, 2048, 2048, 128, 640
        Dc = 4 * D
        return {
            "x": _w((lambda *s: torch.randn(*s, dtype=torch.bfloat16) * 0.8)(B, Sq, M)),
            "c_kv": _w((lambda *s: torch.randn(*s, dtype=torch.bfloat16) * 0.4)(B, Sk, Dc)),
            "w_q": _w((lambda *s: torch.randn(*s, dtype=torch.bfloat16) * 6.4)(M, H, D) * (M ** -0.5)),
            "w_uk": _w((lambda *s: torch.randn(*s, dtype=torch.bfloat16) * 6.4)(Dc, H, D) * (Dc ** -0.5)),
            "w_uv": _w((lambda *s: torch.randn(*s, dtype=torch.bfloat16) * 0.4)(Dc, H, D) * (Dc ** -0.5)),
            "w_o": _w((lambda *s: torch.randn(*s, dtype=torch.bfloat16) * 0.4)(H, D, M) * ((H * D) ** -0.5)),
        }
    _A_in = _A_setup()

    def _B_setup():
        B, S, D, H = 1, 2048, 640, 32
        Dh = D // H
        return {
            "x": _w((lambda *s: torch.randn(*s, dtype=torch.bfloat16) * 0.4)(B, S, D)),
            "wqkv": _w((lambda *s: torch.randn(*s, dtype=torch.bfloat16) * 0.4)(D, 3 * D)),
            "w_phi": _w((lambda *s: torch.randn(*s, dtype=torch.bfloat16) * 0.4)(H, Dh, Dh) * (Dh ** -0.5)),
            "wo": _w((lambda *s: torch.randn(*s, dtype=torch.bfloat16) * 0.05)(D, D)),
        }
    _B_in = _B_setup()

    return {
        "A_x": _A_in["x"],
        "A_c_kv": _A_in["c_kv"],
        "A_w_q": _A_in["w_q"],
        "A_w_uk": _A_in["w_uk"],
        "A_w_uv": _A_in["w_uv"],
        "A_w_o": _A_in["w_o"],
        "B_x": _B_in["x"],
        "B_wqkv": _B_in["wqkv"],
        "B_w_phi": _B_in["w_phi"],
        "B_wo": _B_in["wo"],
    }

def reference(A_x, A_c_kv, A_w_q, A_w_uk, A_w_uv, A_w_o, B_x, B_wqkv, B_w_phi, B_wo):
    def _A_ref(x, c_kv, w_q, w_uk, w_uv, w_o):
        B, Sq, M = x.shape[0], x.shape[1], x.shape[2]
        Sk = c_kv.shape[1]
        H, D = w_q.shape[1], w_q.shape[2]
        q = torch.einsum("bqm,mhd->bhqd", x, w_q)
        k = torch.einsum("bkc,chd->bhkd", c_kv, w_uk)
        v = torch.einsum("bkc,chd->bhkd", c_kv, w_uv)
        scores = torch.einsum("bhqd,bhkd->bhqk", q, k) * (D ** -0.5)
        off = Sk - Sq
        qi = torch.arange(Sq)[:, None]
        kj = torch.arange(Sk)[None, :]
        cmask = kj <= (qi + off)
        scores = torch.where(cmask[None, None], scores, -1e9)
        p = _softmax(scores, axis=-1)
        ctx = torch.einsum("bhqk,bhkd->bhqd", p, v)
        return torch.einsum("bhqd,hdm->bqm", ctx, w_o)

    def _B_ref(x, wqkv, w_phi, wo):
        B, S, D = x.shape
        H = 32; Dh = D // H
        qkv = (lambda x, *d: x.permute(*d))((x @ wqkv).reshape(B, S, 3, H, Dh), 2, 0, 3, 1, 4)
        q, k, v = qkv[0], qkv[1], qkv[2]
        pq = torch.einsum("bhsd,hdf->bhsf", q, w_phi)
        pk = torch.einsum("bhsd,hdf->bhsf", k, w_phi)
        phi_q = torch.concatenate([_softmax(pq, axis=-1), _softmax(-pq, axis=-1)], axis=-1)
        phi_k = torch.concatenate([_softmax(pk, axis=-1), _softmax(-pk, axis=-1)], axis=-1)
        kv = torch.einsum("bhsf,bhsd->bhfd", phi_k, v)
        z  = torch.einsum("bhsf->bhf", phi_k)
        num = torch.einsum("bhqf,bhfd->bhqd", phi_q, kv)
        den = torch.einsum("bhqf,bhf->bhq", phi_q, z)[..., None] + 1e-6
        o = num / den
        return (lambda x, *d: x.permute(*d))(o, 0, 2, 1, 3).reshape(B, S, D) @ wo

    a = _A_ref(A_x, A_c_kv, A_w_q, A_w_uk, A_w_uv, A_w_o)
    b = _B_ref(B_x, B_wqkv, B_w_phi, B_wo)
    return torch.concatenate([a, b], axis=-1)
```

```cpp
#include <hip/hip_runtime.h>
#include <hip/hip_bf16.h>
#include <stdint.h>
typedef unsigned short u16;
__device__ inline float bf2f(u16 v){ return __uint_as_float(((unsigned)v)<<16); }
__device__ inline u16 f2bf(float f){ unsigned u=__float_as_uint(f); u += 0x7FFFu + ((u>>16)&1u); return (u16)(u>>16); }

__global__ void gemm_naive(const u16* A,int lda,const u16* B,int ldb,u16* C,int ldc,int M,int N,int K,float scale){
  __shared__ float As[32][33], Bs[32][33];
  int tx=threadIdx.x, ty=threadIdx.y; int m=blockIdx.y*32+ty, n=blockIdx.x*32+tx; float acc=0;
  for(int k0=0;k0<K;k0+=32){
    As[ty][tx]=bf2f(A[(size_t)m*lda+k0+tx]);
    Bs[ty][tx]=(n<N)?bf2f(B[(size_t)(k0+ty)*ldb+n]):0.f;
    __syncthreads();
    for(int k=0;k<32;k++) acc+=As[ty][k]*Bs[k][tx];
    __syncthreads();
  }
  if(n<N) C[(size_t)m*ldc+n]=f2bf(acc*scale);
}
__global__ void attn_naive(const u16* Q,const u16* K,const u16* V,u16* O){
  int row=blockIdx.x, h=blockIdx.y, t=threadIdx.x; __shared__ float p[2048]; __shared__ float q[128]; __shared__ float red[128];
  q[t]=bf2f(Q[(size_t)row*4096+h*128+t]); __syncthreads();
  float mx=-1e30f;
  for(int j=t;j<=row;j+=128){ const u16* k=K+(size_t)j*4096+h*128; float s=0; for(int d=0;d<128;d++) s+=q[d]*bf2f(k[d]); s*=0.08838834764831845f; p[j]=s; mx=fmaxf(mx,s);}
  red[t]=mx; __syncthreads(); for(int o=64;o>0;o>>=1){ if(t<o) red[t]=fmaxf(red[t],red[t+o]); __syncthreads(); } mx=red[0]; __syncthreads();
  float sum=0; for(int j=t;j<=row;j+=128){ float e=__expf(p[j]-mx); p[j]=e; sum+=e; }
  red[t]=sum; __syncthreads(); for(int o=64;o>0;o>>=1){ if(t<o) red[t]+=red[t+o]; __syncthreads(); } sum=red[0];
  float acc=0; for(int j=0;j<=row;j++) acc+=bf2f(f2bf(p[j]))*bf2f(V[(size_t)j*4096+h*128+t]);
  O[(size_t)row*4096+h*128+t]=f2bf(acc/sum);
}
__device__ inline void phi(const u16* x,const u16* w,float* out){
  float p[20]; float mx=-1e30f,mn=1e30f;
  _Pragma("unroll") for(int f=0;f<20;f++){ float s=0; _Pragma("unroll") for(int d=0;d<20;d++) s+=bf2f(x[d])*bf2f(w[d*20+f]); p[f]=s; mx=fmaxf(mx,s); mn=fminf(mn,s);}
  float s1=0,s2=0; _Pragma("unroll") for(int f=0;f<20;f++){ out[f]=__expf(p[f]-mx); s1+=out[f]; out[20+f]=__expf(mn-p[f]); s2+=out[20+f]; }
  _Pragma("unroll") for(int f=0;f<20;f++){ out[f]/=s1; out[20+f]/=s2; }
}
__global__ __launch_bounds__(64) void b_kv(const u16* qkv,const u16* wphi,float* kv){
  int h=blockIdx.x,t=threadIdx.x; __shared__ float ph[64][41]; __shared__ float vv[64][21];
  float acc[14]; _Pragma("unroll") for(int i=0;i<14;i++) acc[i]=0;
  for(int r0=0;r0<2048;r0+=64){
    int r=r0+t; float o[40]; phi(qkv+(size_t)r*1920+640+h*20,wphi+h*400,o);
    _Pragma("unroll") for(int f=0;f<40;f++) ph[t][f]=o[f];
    for(int d=0;d<20;d++) vv[t][d]=bf2f(qkv[(size_t)r*1920+1280+h*20+d]); vv[t][20]=1.f;
    __syncthreads();
    _Pragma("unroll") for(int i=0;i<14;i++){ int e=t+64*i; if(e<840){ int f=e/21,d=e%21; float s=0; for(int j=0;j<64;j++) s+=ph[j][f]*vv[j][d]; acc[i]+=s; } }
    __syncthreads();
  }
  _Pragma("unroll") for(int i=0;i<14;i++){ int e=t+64*i; if(e<840) kv[h*840+e]=acc[i]; }
}
__global__ __launch_bounds__(64) void b_o(const u16* qkv,const u16* wphi,const float* kv,u16* o){
  int h=blockIdx.y,r=blockIdx.x*64+threadIdx.x; float p[40]; phi(qkv+(size_t)r*1920+h*20,wphi+h*400,p);
  const float* k=kv+h*840; float den=0; _Pragma("unroll") for(int f=0;f<40;f++) den+=p[f]*k[f*21+20]; den+=1e-6f;
  for(int d=0;d<20;d++){ float s=0; _Pragma("unroll") for(int f=0;f<40;f++) s+=p[f]*k[f*21+d]; o[(size_t)r*640+h*20+d]=f2bf(s/den);}
}
extern "C" void kernel_launch(void* const* d_in,const int* in_sizes,int n_in,void* d_out,int out_size,void* d_ws,size_t ws_size,hipStream_t stream){
  const u16* Ax=(const u16*)d_in[0]; const u16* ckv=(const u16*)d_in[1]; const u16* wq=(const u16*)d_in[2]; const u16* wuk=(const u16*)d_in[3];
  const u16* wuv=(const u16*)d_in[4]; const u16* wo=(const u16*)d_in[5]; const u16* Bx=(const u16*)d_in[6]; const u16* wqkv=(const u16*)d_in[7];
  const u16* wphi=(const u16*)d_in[8]; const u16* Bwo=(const u16*)d_in[9]; u16* out=(u16*)d_out;
  char* ws=(char*)d_ws; u16* Q=(u16*)ws; u16* K=Q+2048*4096; u16* V=K+2048*4096; u16* O=V+2048*4096; u16* qkv=O+2048*4096; u16* ob=qkv+2048*1920; float* kv=(float*)(ob+2048*640);
  dim3 b(32,32);
  gemm_naive<<<dim3(4096/32,64),b,0,stream>>>(Ax,640,wq,4096,Q,4096,2048,4096,640,1.f);
  gemm_naive<<<dim3(4096/32,64),b,0,stream>>>(ckv,512,wuk,4096,K,4096,2048,4096,512,1.f);
  gemm_naive<<<dim3(4096/32,64),b,0,stream>>>(ckv,512,wuv,4096,V,4096,2048,4096,512,1.f);
  gemm_naive<<<dim3(1920/32,64),b,0,stream>>>(Bx,640,wqkv,1920,qkv,1920,2048,1920,640,1.f);
  attn_naive<<<dim3(2048,32),128,0,stream>>>(Q,K,V,O);
  b_kv<<<32,64,0,stream>>>(qkv,wphi,kv);
  b_o<<<dim3(32,32),64,0,stream>>>(qkv,wphi,kv,ob);
  gemm_naive<<<dim3(640/32,64),b,0,stream>>>(O,4096,wo,640,out,1280,2048,640,4096,1.f);
  gemm_naive<<<dim3(640/32,64),b,0,stream>>>(ob,640,Bwo,640,out+640,1280,2048,640,640,1.f);
}
```

```cpp
#include <hip/hip_runtime.h>
#include <stdint.h>
typedef unsigned short u16;
typedef short bf16x8 __attribute__((ext_vector_type(8)));
typedef short s16x4 __attribute__((ext_vector_type(4)));
typedef float f32x4 __attribute__((ext_vector_type(4)));
typedef float f32x2 __attribute__((ext_vector_type(2)));
typedef __bf16 bf16x2_t __attribute__((ext_vector_type(2)));
typedef unsigned u32x4 __attribute__((ext_vector_type(4)));
typedef unsigned u32x2 __attribute__((ext_vector_type(2)));
typedef __amdgpu_buffer_rsrc_t rsrc_t;
#define LAS __attribute__((address_space(3)))
#define DI __device__ __forceinline__
#define SBAR() __builtin_amdgcn_sched_barrier(0)
#define PIN(x) asm volatile("" : "+v"(x))
#define MFMA16(a, b, c) __builtin_amdgcn_mfma_f32_16x16x32_bf16(a, b, c, 0, 0, 0)
DI void mfma_acc(f32x4& acc, const bf16x8& a, const bf16x8& b) { asm volatile("v_mfma_f32_16x16x32_bf16 %0, %1, %2, %0" : "+v"(acc) : "v"(a), "v"(b)); }
DI void mfma_init(f32x4& d, const bf16x8& a, const bf16x8& b, const f32x4& c) { asm volatile("v_mfma_f32_16x16x32_bf16 %0, %1, %2, %3" : "=&v"(d) : "v"(a), "v"(b), "v"(c)); }
DI void mfma_zero(f32x4& d, const bf16x8& a, const bf16x8& b) { asm volatile("v_mfma_f32_16x16x32_bf16 %0, %1, %2, 0" : "=&v"(d) : "v"(a), "v"(b)); }
#define MFMA_SETTLE() do { SBAR(); asm volatile("s_nop 7\n\ts_nop 1" ::: "memory"); SBAR(); } while (0)
DI unsigned rfl(unsigned x) { return __builtin_amdgcn_readfirstlane(x); }
DI unsigned cvtpk(float lo, float hi) { f32x2 v = {lo, hi}; bf16x2_t b = __builtin_convertvector(v, bf16x2_t); return __builtin_bit_cast(unsigned, b); }
DI float bf_lo(unsigned w) { return __uint_as_float(w << 16); }
DI float bf_hi(unsigned w) { return __uint_as_float(w & 0xffff0000u); }
DI rsrc_t mk_rsrc(const void* p, unsigned bytes) { return __builtin_amdgcn_make_buffer_rsrc((void*)p, (short)0, (int)bytes, 0x00020000); }
DI bf16x8 lds_rd128(const LAS char* p) { return *(const LAS bf16x8*)p; }
typedef short v4i16_t __attribute__((ext_vector_type(4)));
DI s16x4 lds_rdtr(const LAS char* p) { return __builtin_bit_cast(s16x4, __builtin_amdgcn_ds_read_tr16_b64_v4i16((LAS v4i16_t*)p)); }
DI float max3f(float a, float b, float c) { float r; asm("v_max3_f32 %0, %1, %2, %3" : "=v"(r) : "v"(a), "v"(b), "v"(c)); return r; }
DI float fadd1(float a, float b) { float r; asm("v_add_f32_e32 %0, %1, %2" : "=v"(r) : "v"(a), "v"(b)); return r; }
DI void set_m0(unsigned a) { asm volatile("s_mov_b32 m0, %0\n\ts_nop 0" ::"s"(a) : "memory"); }
template <int OFF> DI void dma16m(unsigned voff, rsrc_t srd, unsigned soff) {
  asm volatile("buffer_load_dwordx4 %0, %1, %2 offen offset:%c3 lds" ::"v"(voff), "s"(srd), "s"(soff), "i"(OFF) : "memory");
}
DI void dma16(unsigned ldsaddr, unsigned voff, rsrc_t srd, unsigned soff) {
  asm volatile("s_mov_b32 m0, %0\n\ts_nop 0\n\tbuffer_load_dwordx4 %1, %2, %3 offen lds" ::"s"(ldsaddr), "v"(voff), "s"(srd), "s"(soff) : "memory");
}
DI float xrow_max(float x) {
  auto a = __builtin_amdgcn_permlane32_swap(__float_as_uint(x), __float_as_uint(x), false, false); x = fmaxf(__uint_as_float(a[0]), __uint_as_float(a[1]));
  auto b = __builtin_amdgcn_permlane16_swap(__float_as_uint(x), __float_as_uint(x), false, false); return fmaxf(__uint_as_float(b[0]), __uint_as_float(b[1]));
}
DI float xrow_sum(float x) {
  auto a = __builtin_amdgcn_permlane32_swap(__float_as_uint(x), __float_as_uint(x), false, false); x = __uint_as_float(a[0]) + __uint_as_float(a[1]);
  auto b = __builtin_amdgcn_permlane16_swap(__float_as_uint(x), __float_as_uint(x), false, false); return __uint_as_float(b[0]) + __uint_as_float(b[1]);
}
DI u32x4 pair_swap(unsigned x0, unsigned x1, unsigned y0, unsigned y1) {
  auto a = __builtin_amdgcn_permlane16_swap(x0, y0, false, false);
  auto b = __builtin_amdgcn_permlane16_swap(x1, y1, false, false);
  return (u32x4){a[0], b[0], a[1], b[1]};
}

namespace proj {
constexpr int LDS_X = 0, LDS_W = 65536, LDS_BYTES = 131072;
constexpr float QSCALE = 0.08838834764831845f * 1.4426950408889634f;

struct Src {
  rsrc_t X, W; unsigned vX[4], vW[2]; unsigned sX, sW, wstep, rowX8;
  unsigned rowW2;
};
struct Frag { bf16x8 x[8]; s16x4 wl[2][4], wh[2][4]; };
#define WF(ks, n) __builtin_shufflevector(fr.wl[ks][n], fr.wh[ks][n], 0, 1, 2, 3, 4, 5, 6, 7)
struct Adr { const LAS char* x[2][2]; const LAS char* w[4]; };

enum { U_Q = 0, U_K = 1, U_V = 2, U_B = 3 };
struct Unit { int kind, pm, pn; };

struct Ptrs { const u16 *Ax, *ckv, *Bx, *wq, *wuk, *wuv, *wqkv; };
DI int fresh_lane() { int l; asm volatile("v_mbcnt_lo_u32_b32 %0, -1, 0\n\tv_mbcnt_hi_u32_b32 %0, -1, %0" : "=v"(l)); return l; }
DI void setup_src(Src& s, const Unit& u, const Ptrs& P, int wid) {
  const int lane = fresh_lane();
  const u16 *Ax = P.Ax, *ckv = P.ckv, *Bx = P.Bx, *wq = P.wq, *wuk = P.wuk, *wuv = P.wuv, *wqkv = P.wqkv;
  const int ldx = (u.kind == U_K || u.kind == U_V) ? 512 : 640;
  const u16* X = u.kind == U_Q ? Ax : (u.kind == U_B ? Bx : ckv);
  s.X = mk_rsrc(X, 2048u * (unsigned)ldx * 2u);
  const int r = lane >> 3, f0 = ((r >> 1) & 1) | (((r >> 2) & 1) << 1);
#pragma unroll
  for (int i = 0; i < 4; ++i) s.vX[i] = (unsigned)(r * ldx * 2) + (unsigned)((((lane & 7) ^ f0 ^ (((i >> 1) & 1) << 1) ^ ((i & 1) << 2)) & 7) * 16);
  s.rowX8 = (unsigned)(8 * ldx * 2);
  s.sX = (unsigned)((u.pm * 256 + wid * 32) * ldx * 2);
  if (u.kind == U_B) {
    s.W = mk_rsrc(wqkv, 640u * 1920u * 2u);
#pragma unroll
    for (int p = 0; p < 2; ++p) { const int sg = ((2 * p + (lane >> 5)) & 3) | ((wid & 1) << 2); const int ch = (lane & 31) ^ (sg << 1);
      const int col = ch < 10 ? u.pn * 160 + ch * 16 : (ch < 20 ? 1280 + u.pn * 160 + (ch - 10) * 16 : (ch < 30 ? 2560 + u.pn * 160 + (ch - 20) * 16 : u.pn * 160));
      s.vW[p] = (unsigned)((lane >> 5) * 3840 + col); }
    s.rowW2 = 2u * 3840u; s.wstep = 64u * 3840u; s.sW = (unsigned)(wid * 8 * 3840);
  } else {
    const u16* W = u.kind == U_Q ? wq : (u.kind == U_K ? wuk : wuv);
    s.W = mk_rsrc(W, (u.kind == U_Q ? 640u : 512u) * 4096u * 2u);
#pragma unroll
    for (int p = 0; p < 2; ++p) { const int sg = ((2 * p + (lane >> 5)) & 3) | ((wid & 1) << 2); s.vW[p] = (unsigned)((lane >> 5) * 8192 + (((lane & 31) ^ (sg << 1)) * 16)); }
    s.rowW2 = 2u * 8192u; s.wstep = 64u * 8192u; s.sW = (unsigned)(wid * 8 * 8192 + u.pn * 512);
  }
}
template <int J> DI void dma_piece(const Src& s, unsigned ldsw, int slot) {
  if constexpr (J < 4) dma16(ldsw + LDS_X + slot * 32768 + J * 1024, s.vX[J], s.X, s.sX + J * s.rowX8);
  else dma16(ldsw + LDS_W + slot * 32768 + (J - 4) * 1024, s.vW[(J - 4) & 1], s.W, s.sW + (J - 4) * s.rowW2);
}
DI void src_next(Src& s) { s.sX += 128u; s.sW += s.wstep; }

DI void setup_adr(Adr& a, const LAS char* lds, bool kappa, int wr, int wc) {
  const int lane = fresh_lane();
  const int c = lane & 15, g = lane >> 4;
#pragma unroll
  for (int par = 0; par < 2; ++par) {
    const int row = kappa ? 8 * (c >> 2) + 4 * par + (c & 3) : 16 * par + c;
    const int f = ((row >> 1) & 1) | ((((row >> 2) ^ (row >> 4)) & 1) << 1) | (((row >> 3) & 1) << 2);
#pragma unroll
    for (int ks = 0; ks < 2; ++ks) a.x[par][ks] = lds + LDS_X + wr * 16384 + row * 128 + (((4 * ks + g) ^ f) * 16);
  }
  const int q = (lane & 15) >> 2, p = lane & 3, sg = q | ((g & 1) << 2);
#pragma unroll
  for (int n = 0; n < 4; ++n) a.w[n] = lds + LDS_W + (8 * g + q) * 512 + (((4 * wc + n) ^ sg) * 32) + 8 * p;
}
#define RDX(SL, f) (fr.x[(f) & 7] = lds_rd128(ad.x[(f) & 1][((f) >> 3) & 1] + (SL) * 32768 + (((f) & 7) >> 1) * 4096))
#define RDW(SL, ks, n, h) do { if (h) fr.wh[ks][n] = lds_rdtr(ad.w[n] + (SL) * 32768 + (ks) * 16384 + 2048); else fr.wl[ks][n] = lds_rdtr(ad.w[n] + (SL) * 32768 + (ks) * 16384); } while (0)

template <int SL, bool FIRST, bool SWAP>
DI void ktile(f32x4 (&acc)[8][4], Frag& fr, const Adr& ad, Src& src, const Unit& nu, const Ptrs& P, int wid, bool switch_src, bool do_dma, unsigned ldsw) {
#define MM(f, n) do { \
    if (FIRST && (f) < 8) { if (SWAP) mfma_zero(acc[(f) & 7][n], fr.x[(f) & 7], WF(0, n)); else mfma_zero(acc[(f) & 7][n], WF(0, n), fr.x[(f) & 7]); } \
    else { if (SWAP) mfma_acc(acc[(f) & 7][n], fr.x[(f) & 7], WF((f) >> 3, n)); else mfma_acc(acc[(f) & 7][n], WF((f) >> 3, n), fr.x[(f) & 7]); } SBAR(); } while (0)
#define GRP(f) do { \
    if ((f) == 9) { asm volatile("s_waitcnt vmcnt(0) lgkmcnt(0)\n\ts_barrier" ::: "memory"); SBAR(); if (switch_src) setup_src(src, nu, P, wid); } \
    MM(f, 0); \
    if ((f) < 4) { RDW(SL, 1, (f) & 3, 0); } else if ((f) >= 9 && (f) < 13) { RDW(SL ^ 1, 0, ((f) - 9) & 3, 0); } \
    SBAR(); \
    MM(f, 1); \
    if ((f) < 4) { RDW(SL, 1, (f) & 3, 1); } else if ((f) >= 9 && (f) < 13) { RDW(SL ^ 1, 0, ((f) - 9) & 3, 1); } \
    SBAR(); \
    MM(f, 2); \
    if (do_dma) { if ((f) == 9) dma_piece<0>(src, ldsw, SL); if ((f) == 10) dma_piece<4>(src, ldsw, SL); if ((f) == 11) dma_piece<1>(src, ldsw, SL); if ((f) == 12) dma_piece<5>(src, ldsw, SL); \
                  if ((f) == 13) dma_piece<2>(src, ldsw, SL); if ((f) == 14) dma_piece<6>(src, ldsw, SL); if ((f) == 15) { dma_piece<3>(src, ldsw, SL); } } \
    SBAR(); \
    MM(f, 3); \
    if ((f) == 15 && do_dma) { dma_piece<7>(src, ldsw, SL); src_next(src); } \
    if ((f) < 8) RDX(SL, (f) + 8); else if ((f) == 9) { RDX(SL ^ 1, 0); SBAR(); RDX(SL ^ 1, 1); } else if ((f) > 9) RDX(SL ^ 1, (f) - 8); \
    SBAR(); } while (0)
  GRP(0); GRP(1); GRP(2); GRP(3); GRP(4); GRP(5); GRP(6); GRP(7); GRP(8); GRP(9); GRP(10); GRP(11); GRP(12); GRP(13); GRP(14); GRP(15);
#undef GRP
#undef MM
}

template <bool SWAP>
DI void run_unit(f32x4 (&acc)[8][4], const Adr& ad, Src& src, const Unit& nu, const Ptrs& P, int wid, bool has_next, int nt, unsigned ldsw) {
  Frag fr;
  if (SWAP) asm volatile("; unit: tokens on rows" ::: "memory"); else asm volatile("; unit: weights on rows" ::: "memory");
  RDW(0, 0, 0, 0); RDW(0, 0, 0, 1); RDX(0, 0); RDW(0, 0, 1, 0); RDW(0, 0, 1, 1); RDW(0, 0, 2, 0); RDW(0, 0, 2, 1); RDW(0, 0, 3, 0); RDW(0, 0, 3, 1);
  RDX(0, 1); RDX(0, 2); RDX(0, 3); RDX(0, 4); RDX(0, 5); RDX(0, 6); RDX(0, 7);
  SBAR();
  ktile<0, true, SWAP>(acc, fr, ad, src, nu, P, wid, false, true, ldsw);
  ktile<1, false, SWAP>(acc, fr, ad, src, nu, P, wid, false, true, ldsw);
  for (int t = 2; t < nt - 2; t += 2) {
    ktile<0, false, SWAP>(acc, fr, ad, src, nu, P, wid, false, true, ldsw);
    ktile<1, false, SWAP>(acc, fr, ad, src, nu, P, wid, false, true, ldsw);
  }
  ktile<0, false, SWAP>(acc, fr, ad, src, nu, P, wid, has_next, has_next, ldsw);
  ktile<1, false, SWAP>(acc, fr, ad, src, nu, P, wid, false, has_next, ldsw);
  MFMA_SETTLE();
}

#define ST_AUX 0
DI void st16(const u32x4& w, rsrc_t r, unsigned voff, unsigned soff) { __builtin_amdgcn_raw_buffer_store_b128(w, r, voff + soff, 0, ST_AUX); }
DI void epi_qk(const f32x4 (&acc)[8][4], const Unit& u, u16* __restrict__ dst, float scale, int wr, int wc) {
  const int lane = fresh_lane();
  const int g = lane >> 4; const unsigned voff = (unsigned)((lane & 15) + 16 * (((g & 1) << 1) | (g >> 1))) * 16u;
  const rsrc_t r = mk_rsrc(dst, 2048u * 4096u * 2u);
  const unsigned base = (unsigned)(((u.pn * 2 + (wc >> 1)) * 128 + u.pm * 16 + wr * 8) * 4 + (wc & 1) * 2) * 1024u;
#pragma unroll
  for (int m = 0; m < 8; ++m)
#pragma unroll
    for (int t = 0; t < 2; ++t) {
      const f32x4 a = acc[m][2 * t] * scale, b = acc[m][2 * t + 1] * scale;
      st16(pair_swap(cvtpk(a[0], a[1]), cvtpk(a[2], a[3]), cvtpk(b[0], b[1]), cvtpk(b[2], b[3])), r, voff + t * 1024, base + m * 4096);
    }
}
DI void epi_v(const f32x4 (&acc)[8][4], const Unit& u, u16* __restrict__ Vf, int wr, int wc) {
  const unsigned voff = (unsigned)fresh_lane() * 16u;
  const rsrc_t r = mk_rsrc(Vf, 2048u * 4096u * 2u);
  const unsigned base = (unsigned)(((u.pn * 2 + (wc >> 1)) * 64 + u.pm * 8 + wr * 4) * 8 + (wc & 1) * 4) * 1024u;
#pragma unroll
  for (int mm = 0; mm < 4; ++mm)
#pragma unroll
    for (int n = 0; n < 4; ++n) {
      const f32x4 a = acc[2 * mm][n], b = acc[2 * mm + 1][n];
      st16((u32x4){cvtpk(a[0], a[1]), cvtpk(a[2], a[3]), cvtpk(b[0], b[1]), cvtpk(b[2], b[3])}, r, voff + n * 1024, base + mm * 8192);
    }
}
DI void epi_braw(const f32x4 (&acc)[8][4], const Unit& u, u16* __restrict__ Braw, int wr, int wc) {
  const int lane = fresh_lane(), g = lane >> 4, gp = ((g & 1) << 1) | (g >> 1);
  const rsrc_t r = mk_rsrc(Braw, 2048u * 2048u * 2u);
  const unsigned voff = (unsigned)((lane & 15) * 4096 + gp * 16), base = (unsigned)((u.pm * 256 + wr * 128) * 4096 + u.pn * 512 + wc * 128);
#pragma unroll
  for (int m = 0; m < 8; ++m)
#pragma unroll
    for (int t = 0; t < 2; ++t) { const f32x4 a = acc[m][2 * t], b = acc[m][2 * t + 1];
      st16(pair_swap(cvtpk(a[0], a[1]), cvtpk(a[2], a[3]), cvtpk(b[0], b[1]), cvtpk(b[2], b[3])), r, voff + t * 64, base + m * 65536); }
}

__global__ __launch_bounds__(512, 2) void proj_kernel(const u16* __restrict__ Ax, const u16* __restrict__ ckv, const u16* __restrict__ wq, const u16* __restrict__ wuk, const u16* __restrict__ wuv,
                                                      const u16* __restrict__ Bx, const u16* __restrict__ wqkv, u16* __restrict__ Qf, u16* __restrict__ Kf, u16* __restrict__ Vf,
                                                      u16* __restrict__ Braw, unsigned long long* dbg) {
  extern __shared__ __attribute__((aligned(16))) char smem[];
  const LAS char* lds = (const LAS char*)smem;
  const int tid = threadIdx.x, lane = tid & 63, wid = (int)rfl(tid >> 6), wr = wid >> 2, wc = wid & 3;
  const unsigned ldsw = (unsigned)(uintptr_t)lds + (unsigned)wid * 4096u;
  const int x = blockIdx.x & 7, j = blockIdx.x >> 3;
  auto kv_unit = [&](int k) { Unit u; u.kind = (k & 2) ? U_V : U_K; u.pn = 2 * x + (k & 1); u.pm = k >> 2; return u; };
  Unit u0, u1; bool two = true;
  if (j < 16) { u0.kind = U_Q; u0.pn = 2 * x + (j & 1); u0.pm = j >> 1; u1 = kv_unit(j); }
  else if (j < 24) { u0 = kv_unit(16 + 2 * (j - 16)); u1 = kv_unit(17 + 2 * (j - 16)); }
  else { u0.kind = U_B; u0.pn = x; u0.pm = j - 24; u1 = u0; two = false; }
  const Ptrs P = {Ax, ckv, Bx, wq, wuk, wuv, wqkv};
  Src src;
  setup_src(src, u0, P, wid);
  dma_piece<0>(src, ldsw, 0); dma_piece<4>(src, ldsw, 0); dma_piece<1>(src, ldsw, 0); dma_piece<5>(src, ldsw, 0); dma_piece<2>(src, ldsw, 0); dma_piece<6>(src, ldsw, 0); dma_piece<3>(src, ldsw, 0); dma_piece<7>(src, ldsw, 0); src_next(src);
  dma_piece<0>(src, ldsw, 1); dma_piece<4>(src, ldsw, 1); dma_piece<1>(src, ldsw, 1); dma_piece<5>(src, ldsw, 1); dma_piece<2>(src, ldsw, 1); dma_piece<6>(src, ldsw, 1); dma_piece<3>(src, ldsw, 1); dma_piece<7>(src, ldsw, 1); src_next(src);
  f32x4 acc[8][4]; Adr ad;
  Unit u = u0;
  asm volatile("s_waitcnt vmcnt(8)\n\ts_barrier" ::: "memory");
  SBAR();
  for (int ui = 0; ui < 2; ++ui) {
    const bool has_next = two && ui == 0;
    const int nt = (u.kind == U_K || u.kind == U_V) ? 8 : 10;
    setup_adr(ad, lds, u.kind == U_K || u.kind == U_V, wr, wc);
    if (u.kind == U_V) run_unit<true>(acc, ad, src, u1, P, wid, has_next, nt, ldsw);
    else run_unit<false>(acc, ad, src, u1, P, wid, has_next, nt, ldsw);
    if (u.kind == U_V) epi_v(acc, u, Vf, wr, wc);
    else if (u.kind == U_B) epi_braw(acc, u, Braw, wr, wc);
    else epi_qk(acc, u, u.kind == U_Q ? Qf : Kf, u.kind == U_Q ? QSCALE : 1.f, wr, wc);
    if (!has_next) break;
    u = u1;
  }
  asm volatile("s_waitcnt vmcnt(0)" ::: "memory");
}
}

#define MFMASUM 1
namespace attn {
constexpr int LDS_K = 0, LDS_V = 65536, LDS_X = 131072, LDS_BYTES = 163840;
constexpr float L_LIMIT = 1.2676506e30f;

struct Til { f32x4 s[8]; };
struct St {
  f32x4 o[16];
  bf16x8 q[8];
  f32x4 negm[2];
  bf16x8 fr[8];
  u32x4 pw[4];
  f32x2 l[2];
  f32x4 ls[2]; bf16x8 ones;
  float m[2];
};

DI void cmask(Til& t, int thr) {
  const float NEG = -__builtin_inff();
#pragma unroll
  for (int kb = 0; kb < 4; ++kb)
#pragma unroll
    for (int qb = 0; qb < 2; ++qb)
#pragma unroll
      for (int r = 0; r < 4; ++r) if (32 * (kb >> 1) + 4 * (kb & 1) + r - 16 * qb > thr) t.s[2 * kb + qb][r] = NEG;
}

DI void safe_path(const u16* __restrict__ Qf, const u16* __restrict__ Kf, const u16* __restrict__ Vf, u16* __restrict__ Cf, int head, int blk, int rg, int grp, int lane) {
  const int g = lane >> 4, c = lane & 15;
  for (int qb = 0; qb < 2; ++qb) {
    const bf16x8* qp = (const bf16x8*)Qf + ((size_t)(head * 64 + blk * 4 + rg) * 8 + qb * 4) * 64 + lane;
    bf16x8 q[4];
#pragma unroll
    for (int d = 0; d < 4; ++d) q[d] = qp[d * 64];
    f32x4 o[4] = {};
    float m = -1e30f, l = 0.f;
    const int row = blk * 128 + rg * 32 + qb * 16 + c;
    const int nsteps = blk * 4 + rg + 1;
    for (int ks = 0; ks < nsteps; ++ks) {
      const bf16x8* kp = (const bf16x8*)Kf + ((size_t)(head * 32 + (ks >> 1)) * 16 + (ks & 1) * 8) * 64 + lane;
      f32x4 s[2] = {};
      float mx = -__builtin_inff();
#pragma unroll
      for (int b = 0; b < 2; ++b) {
#pragma unroll
        for (int d = 0; d < 4; ++d) s[b] = MFMA16(kp[(b * 4 + d) * 64], q[d], s[b]);
#pragma unroll
        for (int r = 0; r < 4; ++r) { if (ks * 32 + 8 * g + 4 * b + r > row) s[b][r] = -__builtin_inff(); mx = fmaxf(mx, s[b][r]); }
      }
      mx = xrow_max(mx);
      const float mn = fmaxf(m, mx), al = __builtin_amdgcn_exp2f(m - mn); m = mn;
      float ps = 0.f;
#pragma unroll
      for (int b = 0; b < 2; ++b)
#pragma unroll
        for (int r = 0; r < 4; ++r) { s[b][r] = __builtin_amdgcn_exp2f(s[b][r] - mn); ps += s[b][r]; }
      l = l * al + ps;
      const u32x4 w = {cvtpk(s[0][0], s[0][1]), cvtpk(s[0][2], s[0][3]), cvtpk(s[1][0], s[1][1]), cvtpk(s[1][2], s[1][3])};
      const bf16x8* vp = (const bf16x8*)Vf + ((size_t)(head * 32 + (ks >> 1)) * 16 + (ks & 1) * 8 + grp * 4) * 64 + lane;
#pragma unroll
      for (int db = 0; db < 4; ++db) { o[db] *= al; o[db] = MFMA16(vp[db * 64], __builtin_bit_cast(bf16x8, w), o[db]); }
    }
    const float il = 1.f / xrow_sum(l);
    u32x4* cp = (u32x4*)Cf + ((size_t)(blk * 8 + rg * 2 + qb) * 128 + head * 4 + grp * 2) * 64 + (c + 16 * (((g & 1) << 1) | (g >> 1)));
#pragma unroll
    for (int t = 0; t < 2; ++t)
      cp[t * 64] = pair_swap(cvtpk(o[2 * t][0] * il, o[2 * t][1] * il), cvtpk(o[2 * t][2] * il, o[2 * t][3] * il), cvtpk(o[2 * t + 1][0] * il, o[2 * t + 1][1] * il), cvtpk(o[2 * t + 1][2] * il, o[2 * t + 1][3] * il));
  }
}

#define A_RDK(n) (st.fr[(n) & 7] = lds_rd128(kp + (n) * 1024))
#define A_RDV(n) (st.fr[(n) & 7] = lds_rd128(vp + (n) * 1024))
#define RDK2(a, b) do { A_RDK(a); SBAR(); A_RDK(b); } while (0)
#define RDV2(a, b) do { A_RDV(a); SBAR(); A_RDV(b); } while (0)
#define EXP2(x) __builtin_amdgcn_exp2f(x)

#define SUMS(qb, a, b) do { } while (0)
struct Dma {
  unsigned voff; rsrc_t srdK, srdV; unsigned kdst, vdst, ksoff, vsoff; };
#define DMA_PIECE(d, j) do { if ((j) == 0) set_m0((d).kdst); if ((j) == 4) set_m0((d).vdst); \
    if ((j) < 4) dma16m<((j) & 3) * 1024>((d).voff, (d).srdK, (d).ksoff); else dma16m<((j) & 3) * 1024>((d).voff, (d).srdV, (d).vsoff); } while (0)
#define XBARL() XBARN(0)
#define XBARN(N) do { SBAR(); asm volatile("s_waitcnt vmcnt(" #N ") lgkmcnt(0)\n\ts_barrier" ::: "memory"); SBAR(); } while (0)
#define XBAR() XBARN(0)

struct Ctx {
  const u16 *Qf, *Kf, *Vf; u16* Cf;
  const LAS char *lds, *kbase, *vbase;
  unsigned lds0, wpiece;
  int lane, wid, grp, rg, head, T1, g;
};
DI void load_q(St& st, const Ctx& c, int blk) {
  const bf16x8* qp = (const bf16x8*)c.Qf + ((size_t)(c.head * 64 + blk * 4 + c.rg) * 8) * 64 + c.lane;
#pragma unroll
  for (int i = 0; i < 8; ++i) st.q[i] = qp[i * 64];
}
#define PIN_Q() asm volatile("" : "+v"(st.q[0]), "+v"(st.q[1]), "+v"(st.q[2]), "+v"(st.q[3]), "+v"(st.q[4]), "+v"(st.q[5]), "+v"(st.q[6]), "+v"(st.q[7]))
DI void dma_advance(Dma& d, const Ctx& c, int gg) {
  d.kdst ^= 32768u; d.vdst ^= 32768u; d.vsoff = d.ksoff;
  d.ksoff = (gg + 2 == c.T1) ? c.wpiece : d.ksoff + 32768u;
}

template <bool FIRST>
DI void phaseA(St& st, Til& C, const Til& P, const LAS char* kp, const LAS char* vp, const Dma& d) {
#define GA(i) do { \
    if (((i) & 3) == 0) { if (FIRST) mfma_zero(C.s[2 * ((i) >> 2)], st.fr[(i) & 7], st.q[0]); else mfma_init(C.s[2 * ((i) >> 2)], st.fr[(i) & 7], st.q[0], st.negm[0]); } \
    else mfma_acc(C.s[2 * ((i) >> 2)], st.fr[(i) & 7], st.q[(i) & 3]); \
    SBAR(); \
    if (!FIRST) {   \
      const float a_ = P.s[2 * (2 * ((i) >> 3) + (((i) & 3) >> 1)) + (((i) >> 2) & 1)][2 * ((i) & 1)], b_ = P.s[2 * (2 * ((i) >> 3) + (((i) & 3) >> 1)) + (((i) >> 2) & 1)][2 * ((i) & 1) + 1]; \
      { unsigned w_ = cvtpk(a_, b_); PIN(w_); st.pw[(i) >> 2][(i) & 3] = w_; } \
      SUMS(((i) >> 2) & 1, a_, b_); SBAR(); } \
    if (((i) & 3) == 0) { if (FIRST) mfma_zero(C.s[2 * ((i) >> 2) + 1], st.fr[(i) & 7], st.q[4]); else mfma_init(C.s[2 * ((i) >> 2) + 1], st.fr[(i) & 7], st.q[4], st.negm[1]); } \
    else mfma_acc(C.s[2 * ((i) >> 2) + 1], st.fr[(i) & 7], st.q[4 + ((i) & 3)]); \
    SBAR(); \
    if (((i) & 1) == 1) { if ((i) < 8) { RDK2(((i) + 8) & 15, ((i) + 7) & 15); } else if (!FIRST) { RDV2(((i) + 8) & 15, ((i) + 7) & 15); } } \
    if (!FIRST) { if ((i) == 0) DMA_PIECE(d, 3); if ((i) == 2) DMA_PIECE(d, 4); if ((i) == 4) DMA_PIECE(d, 5); if ((i) == 8) DMA_PIECE(d, 6); if ((i) == 10) DMA_PIECE(d, 7); } \
    SBAR(); } while (0)
  GA(0); GA(1); GA(2); GA(3); GA(4); GA(5); GA(6); GA(7); GA(8); GA(9); GA(10); GA(11); GA(12); GA(13); GA(14); GA(15);
#undef GA
}

#define MSUM(i) do { if (((i) & 7) == 2) mfma_acc(st.ls[0], st.ones, __builtin_bit_cast(bf16x8, st.pw[2 * ((i) >> 3)])); if (((i) & 7) == 5) mfma_acc(st.ls[1], st.ones, __builtin_bit_cast(bf16x8, st.pw[2 * ((i) >> 3) + 1])); } while (0)
template <class CTX> DI void phaseB(St& st, Til& C, const LAS char* vp, const LAS char* kn, Dma& d, const CTX& c, int g) {
#define GB(i) do { \
    if ((i) == 11) { XBARL(); dma_advance(d, c, g); } \
    mfma_acc(st.o[2 * ((i) & 7)], st.fr[(i) & 7], __builtin_bit_cast(bf16x8, st.pw[2 * ((i) >> 3)])); \
    SBAR(); \
    { float e_ = EXP2(C.s[(i) >> 1][2 * ((i) & 1)]); PIN(e_); C.s[(i) >> 1][2 * ((i) & 1)] = e_; } \
    SBAR(); \
    mfma_acc(st.o[2 * ((i) & 7) + 1], st.fr[(i) & 7], __builtin_bit_cast(bf16x8, st.pw[2 * ((i) >> 3) + 1])); \
    SBAR(); \
    { float e_ = EXP2(C.s[(i) >> 1][2 * ((i) & 1) + 1]); PIN(e_); C.s[(i) >> 1][2 * ((i) & 1) + 1] = e_; } \
    SBAR(); \
    MSUM(i); \
    if (((i) & 1) == 1 && (i) < 8) { RDV2(((i) + 8) & 15, ((i) + 7) & 15); } \
    if ((i) == 11) { { const LAS char* kp = kn; RDK2(1, 0); SBAR(); RDK2(3, 2); } } \
    if ((i) == 12) { DMA_PIECE(d, 0); } \
    if ((i) == 13) { { const LAS char* kp = kn; RDK2(5, 4); } } \
    if ((i) == 14) { DMA_PIECE(d, 1); DMA_PIECE(d, 2); } \
    if ((i) == 15) { { const LAS char* kp = kn; RDK2(7, 6); } } \
    SBAR(); } while (0)
  GB(0); GB(1); GB(2); GB(3); GB(4); GB(5); GB(6); GB(7); GB(8); GB(9); GB(10); GB(11); GB(12); GB(13); GB(14); GB(15);
#undef GB
}

template <int BI>
DI void do_block(St& st, Dma& d, Ctx& c, const int blk, const int next_blk) {
  const int T = blk + 1, lane = c.lane, grp = c.grp, rg = c.rg, wid = c.wid;
  int g = c.g;
  const int thr = rg * 32 + (lane & 15) - grp * 64 - 8 * (lane >> 4);
  Til a, b;
  { const LAS char* kp = c.kbase + (g & 1) * 32768;
    A_RDK(1); A_RDK(0); A_RDK(3); A_RDK(2); A_RDK(5); A_RDK(4); A_RDK(7); A_RDK(6);
    phaseA<true>(st, a, a, kp, kp, d);
    MFMA_SETTLE();
    if (T == 1) cmask(a, thr);
#pragma unroll
    for (int qb = 0; qb < 2; ++qb) {
      float mx = max3f(a.s[qb][0], a.s[qb][1], a.s[qb][2]), my = max3f(a.s[qb][3], a.s[2 + qb][0], a.s[2 + qb][1]);
      mx = max3f(mx, a.s[2 + qb][2], a.s[2 + qb][3]); my = max3f(my, a.s[4 + qb][0], a.s[4 + qb][1]);
      mx = max3f(mx, a.s[4 + qb][2], a.s[4 + qb][3]); my = max3f(my, a.s[6 + qb][0], a.s[6 + qb][1]);
      mx = max3f(mx, a.s[6 + qb][2], a.s[6 + qb][3]); mx = max3f(mx, my, -1e30f);
      st.m[qb] = xrow_max(mx); st.l[qb] = (f32x2){0.f, 0.f}; st.ls[qb] = (f32x4){};
    }
    XBAR();
    dma_advance(d, c, g);
    DMA_PIECE(d, 0); DMA_PIECE(d, 1); DMA_PIECE(d, 2);
    { const LAS char* kp = c.kbase + ((g + 1) & 1) * 32768; A_RDK(1); A_RDK(0); A_RDK(3); A_RDK(2); A_RDK(5); A_RDK(4); A_RDK(7); A_RDK(6); }
#pragma unroll
    for (int qb = 0; qb < 2; ++qb) { st.negm[qb] = (f32x4){-st.m[qb], -st.m[qb], -st.m[qb], -st.m[qb]};
#pragma unroll
      for (int kb = 0; kb < 4; ++kb)
#pragma unroll
        for (int r = 0; r < 4; ++r) a.s[2 * kb + qb][r] = EXP2(a.s[2 * kb + qb][r] - st.m[qb]); }
#pragma unroll
    for (int i = 0; i < 16; ++i) st.o[i] = (f32x4){};
    ++g;
  }
#define STEP(C, P, t, kp_, vp_, kn_) do { \
    phaseA<false>(st, C, P, kp_, vp_, d); \
    if ((t) == T - 1) { MFMA_SETTLE(); cmask(C, thr); } \
    SBAR(); \
    phaseB(st, C, vp_, kn_, d, c, g); \
    ++g; } while (0)
  int t = 1;
  const LAS char* kE = c.kbase + (g & 1) * 32768; const LAS char* kO = c.kbase + ((g + 1) & 1) * 32768;
  const LAS char* vE = c.vbase + ((g + 1) & 1) * 32768; const LAS char* vO = c.vbase + (g & 1) * 32768;
  for (; t + 1 < T; t += 2) { STEP(b, a, t, kE, vE, kO); STEP(a, b, t + 1, kO, vO, kE); }
  if (t < T) { STEP(b, a, t, kE, vE, kO); a = b; }
#undef STEP
  DMA_PIECE(d, 3); DMA_PIECE(d, 4); DMA_PIECE(d, 5); DMA_PIECE(d, 6); DMA_PIECE(d, 7);
  if (BI == 0) load_q(st, c, next_blk);
  { const LAS char* vp = c.vbase + ((g - 1) & 1) * 32768;
    A_RDV(1); A_RDV(0); A_RDV(3); A_RDV(2); A_RDV(5); A_RDV(4); A_RDV(7); A_RDV(6);
#pragma unroll
    for (int i = 0; i < 16; ++i) { const float x = a.s[2 * (2 * (i >> 3) + ((i & 3) >> 1)) + ((i >> 2) & 1)][2 * (i & 1)], y = a.s[2 * (2 * (i >> 3) + ((i & 3) >> 1)) + ((i >> 2) & 1)][2 * (i & 1) + 1];
      st.pw[i >> 2][i & 3] = cvtpk(x, y);
    }
#define GD(i) do { mfma_acc(st.o[2 * ((i) & 7)], st.fr[(i) & 7], __builtin_bit_cast(bf16x8, st.pw[2 * ((i) >> 3)])); \
      mfma_acc(st.o[2 * ((i) & 7) + 1], st.fr[(i) & 7], __builtin_bit_cast(bf16x8, st.pw[2 * ((i) >> 3) + 1])); MSUM(i); \
      if (((i) & 1) == 1 && (i) < 8) { RDV2(((i) + 8) & 15, ((i) + 7) & 15); } SBAR(); } while (0)
    asm volatile("s_nop 1" : "+v"(st.pw[0]), "+v"(st.pw[1]), "+v"(st.pw[2]), "+v"(st.pw[3]));
    GD(0); GD(1); GD(2); GD(3); GD(4); GD(5); GD(6); GD(7); GD(8); GD(9); GD(10); GD(11); GD(12); GD(13); GD(14); GD(15);
    MFMA_SETTLE();
#undef GD
  }
  float l[2] = {st.ls[0][0], st.ls[1][0]};
  LAS char* xr = (LAS char*)c.lds + LDS_X;
  *(LAS f32x4*)(xr + wid * 1024 + lane * 16) = (f32x4){st.m[0], l[0], st.m[1], l[1]};
  asm volatile("s_waitcnt lgkmcnt(0)\n\ts_barrier" ::: "memory");
  const f32x4 oth = *(const LAS f32x4*)(xr + (wid ^ 4) * 1024 + lane * 16);
  bool bad = false; float fs[2];
#pragma unroll
  for (int qb = 0; qb < 2; ++qb) {
    const float M = fmaxf(st.m[qb], oth[2 * qb]);
    const float fo = EXP2(st.m[qb] - M), fp = EXP2(oth[2 * qb] - M);
    const float lt = l[qb] * fo + oth[2 * qb + 1] * fp;
    bad = bad || !(l[qb] < L_LIMIT) || !(oth[2 * qb + 1] < L_LIMIT);
    fs[qb] = fo * __builtin_amdgcn_rcpf(lt);
  }
  LAS char* vslot = (LAS char*)c.lds + LDS_V + ((g - 1) & 1) * 32768;
  LAS char* sendp = vslot + wid * 4096 + lane * 16;
  const LAS char* recvp = vslot + (wid ^ 4) * 4096 + lane * 16;
  f32x4 k[8];
#define PACK8(x, y, f) (u32x4){cvtpk((x)[0] * (f), (x)[1] * (f)), cvtpk((x)[2] * (f), (x)[3] * (f)), cvtpk((y)[0] * (f), (y)[1] * (f)), cvtpk((y)[2] * (f), (y)[3] * (f))}
  if (grp == 0) {
#pragma unroll
    for (int i = 0; i < 4; ++i) *(LAS u32x4*)(sendp + i * 1024) = PACK8(st.o[8 + 4 * (i >> 1) + (i & 1)], st.o[8 + 4 * (i >> 1) + 2 + (i & 1)], fs[i & 1]);
#pragma unroll
    for (int i = 0; i < 8; ++i) k[i] = st.o[i];
  } else {
#pragma unroll
    for (int i = 0; i < 4; ++i) *(LAS u32x4*)(sendp + i * 1024) = PACK8(st.o[4 * (i >> 1) + (i & 1)], st.o[4 * (i >> 1) + 2 + (i & 1)], fs[i & 1]);
#pragma unroll
    for (int i = 0; i < 8; ++i) k[i] = st.o[8 + i];
  }
  asm volatile("s_waitcnt lgkmcnt(0)\n\ts_barrier" ::: "memory");
  float amax = 0.f;
  u32x4 res[4];
#pragma unroll
  for (int i = 0; i < 4; ++i) {
    const u32x4 x = *(const LAS u32x4*)(recvp + i * 1024);
    const f32x4 p = k[4 * (i >> 1) + (i & 1)], q = k[4 * (i >> 1) + 2 + (i & 1)]; const float f = fs[i & 1];
    const float v0 = p[0] * f + bf_lo(x[0]), v1 = p[1] * f + bf_hi(x[0]), v2 = p[2] * f + bf_lo(x[1]), v3 = p[3] * f + bf_hi(x[1]);
    const float v4 = q[0] * f + bf_lo(x[2]), v5 = q[1] * f + bf_hi(x[2]), v6 = q[2] * f + bf_lo(x[3]), v7 = q[3] * f + bf_hi(x[3]);
    amax = max3f(amax, __builtin_fabsf(v0), __builtin_fabsf(v1)); amax = max3f(amax, __builtin_fabsf(v2), __builtin_fabsf(v3));
    amax = max3f(amax, __builtin_fabsf(v4), __builtin_fabsf(v5)); amax = max3f(amax, __builtin_fabsf(v6), __builtin_fabsf(v7));
    res[i] = pair_swap(cvtpk(v0, v1), cvtpk(v2, v3), cvtpk(v4, v5), cvtpk(v6, v7));
  }
  bad = bad || !(amax < 3.0e38f);
  if (__builtin_expect(__any(bad), 0)) {
    safe_path(c.Qf, c.Kf, c.Vf, c.Cf, c.head, blk, rg, grp, lane);
  } else {
#pragma unroll
    for (int i = 0; i < 4; ++i) ((u32x4*)c.Cf)[((size_t)(blk * 8 + rg * 2 + (i & 1)) * 128 + c.head * 4 + grp * 2 + (i >> 1)) * 64 + ((lane & 15) + 16 * ((((lane >> 4) & 1) << 1) | (lane >> 5)))] = res[i];
  }
  c.g = g;
}

__global__ __launch_bounds__(512, 2) void attn_kernel(const u16* __restrict__ Qf, const u16* __restrict__ Kf, const u16* __restrict__ Vf, u16* __restrict__ Cf, unsigned long long* dbg) {
  extern __shared__ __attribute__((aligned(16))) char smem[];
  Ctx c; c.Qf = Qf; c.Kf = Kf; c.Vf = Vf; c.Cf = Cf;
  c.lds = (const LAS char*)smem; c.lds0 = (unsigned)(uintptr_t)c.lds;
  const int tid = threadIdx.x; c.lane = tid & 63;
  c.wid = (int)rfl(tid >> 6); c.grp = c.wid >> 2; c.rg = c.wid & 3;
  const int xcd = blockIdx.x & 7, jj = blockIdx.x >> 3, pp = jj & 7; c.head = xcd * 4 + (jj >> 3);
  c.T1 = 16 - pp; c.g = 0;
  c.wpiece = (unsigned)c.wid * 4096u;
  c.kbase = c.lds + LDS_K + c.grp * 16384 + c.lane * 16; c.vbase = c.lds + LDS_V + c.grp * 16384 + c.lane * 16;
  Dma d;
  d.voff = (unsigned)c.lane * 16u;
  d.srdK = mk_rsrc(Kf + (size_t)c.head * 262144, 524288u); d.srdV = mk_rsrc(Vf + (size_t)c.head * 262144, 524288u);
  St st;
  st.ones = (bf16x8){0x3F80, 0x3F80, 0x3F80, 0x3F80, 0x3F80, 0x3F80, 0x3F80, 0x3F80}; PIN(st.ones);
  d.kdst = c.lds0 + LDS_K + c.wpiece; d.ksoff = c.wpiece; d.vdst = c.lds0 + LDS_V + c.wpiece; d.vsoff = c.wpiece;
  DMA_PIECE(d, 0); DMA_PIECE(d, 1); DMA_PIECE(d, 2); DMA_PIECE(d, 3);
  load_q(st, c, 15 - pp);
  PIN_Q();
  XBAR();
  DMA_PIECE(d, 4); DMA_PIECE(d, 5); DMA_PIECE(d, 6); DMA_PIECE(d, 7);
  d.kdst = c.lds0 + LDS_K + 32768 + c.wpiece; d.ksoff = 32768u + c.wpiece;
  DMA_PIECE(d, 0); DMA_PIECE(d, 1); DMA_PIECE(d, 2); DMA_PIECE(d, 3);
  do_block<0>(st, d, c, 15 - pp, pp);
  PIN_Q();
  do_block<1>(st, d, c, pp, pp);
  asm volatile("s_waitcnt vmcnt(0)" ::: "memory");
}
}

namespace bs {
DI float b2f(u16 v) { return __uint_as_float(((unsigned)v) << 16); }
DI void phi(const u16* x, const u16* w, float* out) {
  float p[20]; float mx = -1e30f, mn = 1e30f;
#pragma unroll
  for (int f = 0; f < 20; f++) { float s = 0;
#pragma unroll
    for (int d = 0; d < 20; d++) s += b2f(x[d]) * b2f(w[d * 20 + f]);
    p[f] = s; mx = fmaxf(mx, s); mn = fminf(mn, s); }
  float s1 = 0, s2 = 0;
#pragma unroll
  for (int f = 0; f < 20; f++) { out[f] = __expf(p[f] - mx); s1 += out[f]; out[20 + f] = __expf(mn - p[f]); s2 += out[20 + f]; }
#pragma unroll
  for (int f = 0; f < 20; f++) { out[f] /= s1; out[20 + f] /= s2; }
}
__global__ __launch_bounds__(64) void b_kv(const u16* Braw, const u16* wphi, float* part) {
  const int h = blockIdx.x, ch = blockIdx.y, t = threadIdx.x; __shared__ float ph[64][41]; __shared__ float vv[64][21];
  float acc[14];
#pragma unroll
  for (int i = 0; i < 14; i++) acc[i] = 0;
  for (int r0 = ch * 256; r0 < ch * 256 + 256; r0 += 64) {
    const int r = r0 + t; float o[40]; const u16* row = Braw + ((size_t)r * 8 + (h >> 2)) * 256 + (h & 3) * 20;
    phi(row + 80, wphi + h * 400, o);
#pragma unroll
    for (int f = 0; f < 40; f++) ph[t][f] = o[f];
    for (int d = 0; d < 20; d++) vv[t][d] = b2f(row[160 + d]); vv[t][20] = 1.f;
    __syncthreads();
#pragma unroll
    for (int i = 0; i < 14; i++) { int e = t + 64 * i; if (e < 840) { int f = e / 21, d = e % 21; float s = 0; for (int j = 0; j < 64; j++) s += ph[j][f] * vv[j][d]; acc[i] += s; } }
    __syncthreads();
  }
#pragma unroll
  for (int i = 0; i < 14; i++) { int e = t + 64 * i; if (e < 840) part[(ch * 32 + h) * 840 + e] = acc[i]; }
}
__global__ __launch_bounds__(64) void b_o(const u16* Braw, const u16* wphi, const float* part, u16* Of) {
  __shared__ float kv[840];
  const int h = blockIdx.y, r = blockIdx.x * 64 + threadIdx.x;
  for (int e = threadIdx.x; e < 840; e += 64) { float s = 0; for (int c = 0; c < 8; ++c) s += part[(c * 32 + h) * 840 + e]; kv[e] = s; }
  __syncthreads();
  float p[40]; phi(Braw + ((size_t)r * 8 + (h >> 2)) * 256 + (h & 3) * 20, wphi + h * 400, p);
  float den = 0;
#pragma unroll
  for (int f = 0; f < 40; f++) den += p[f] * kv[f * 21 + 20];
  den += 1e-6f;
  for (int d = 0; d < 20; d++) { float s = 0;
#pragma unroll
    for (int f = 0; f < 40; f++) s += p[f] * kv[f * 21 + d];
    const int k = h * 20 + d; const unsigned w = cvtpk(s / den, 0.f);
    Of[(((size_t)(r >> 4) * 20 + (k >> 5)) * 64 + (r & 15) + 16 * ((k >> 3) & 3)) * 8 + (k & 7)] = (u16)(w & 0xffff); }
}
}

namespace outp {
#define NSL 4
constexpr int NSLOT = NSL, DIST = NSL - 1, SLOTB = 20480, LDS_BYTES = NSLOT * SLOTB;
constexpr int RA = 32, RB = 5, NR = RA + RB;

#define XABL(x) (x)
#define WABL(x) (x)
#define XRS 4096u
#define XMA 131072u
#define XMB 20480u
struct Ring { bf16x8 x[NSLOT][2]; };

template <int WM, int YOUNGER> DI void wait_round() { asm volatile("s_waitcnt vmcnt(%0)" ::"i"(YOUNGER * (WM == 0 ? 5 : 4)) : "memory"); }

template <int WM>
DI void body(const u16* __restrict__ Cf, const u16* __restrict__ Of, const u16* __restrict__ Wo, const u16* __restrict__ WoB, u16* __restrict__ out, const LAS char* lds, int pm, int pn, int wk, int lane) {
  const unsigned lds0 = (unsigned)(uintptr_t)lds;
  const rsrc_t rC = mk_rsrc(Cf, 2048u * 4096u * 2u), rO = mk_rsrc(Of, 2048u * 640u * 2u), rW = mk_rsrc(Wo, 4096u * 640u * 2u), rWB = mk_rsrc(WoB, 640u * 640u * 2u);
  constexpr int NP = WM == 0 ? 3 : 2, P0 = WM == 0 ? 0 : 3;
  unsigned vW[NP];
#pragma unroll
  for (int p = 0; p < NP; ++p) { const int idx = (P0 + p) * 64 + lane, r = idx / 10, ch = idx - r * 10, k = (r & ~12) | ((r & 4) << 1) | ((r & 8) >> 1);
    vW[p] = (unsigned)(k * 1280 + pn * 160 + ch * 16); }
  const unsigned vX = (unsigned)lane * 16u;
  const unsigned xA = (unsigned)((pm * 4 + WM * 2) * 128 + wk) * 1024u, xB = (unsigned)((pm * 4 + WM * 2) * 20 + wk) * 1024u;
  const unsigned ldsW = lds0 + (unsigned)wk * 5120u + P0 * 1024u;
  Ring rg;
#define ISSUE(r, slot) do { const int r_ = (r); const bool isA = r_ < RA; const int rb = r_ - RA; \
    const rsrc_t rx_ = isA ? rC : rO, rw_ = isA ? rW : rWB; \
    const unsigned sx0 = isA ? xA + (unsigned)r_ * XRS : xB + (unsigned)rb * XRS, sx1 = sx0 + (isA ? XMA : XMB), sw = (unsigned)((isA ? r_ : rb) * 128 + wk * 32) * 1280u; \
    asm volatile("buffer_load_dwordx4 %0, %1, %2, %3 offen" : "=v"(rg.x[slot][0]) : "v"(vX), "s"(rx_), "s"(XABL(sx0)) : "memory"); \
    asm volatile("buffer_load_dwordx4 %0, %1, %2, %3 offen" : "=v"(rg.x[slot][1]) : "v"(vX), "s"(rx_), "s"(XABL(sx1)) : "memory"); \
    _Pragma("unroll") for (int p = 0; p < NP; ++p) dma16(ldsW + (slot) * SLOTB + p * 1024, vW[p], rw_, WABL(sw)); } while (0)
  const int g = lane >> 4, q = (lane & 15) >> 2, p4 = lane & 3;
  const LAS char* wrd = lds + wk * 5120 + (16 * (g >> 1) + 4 * (g & 1) + q) * 160 + 8 * p4;
  f32x4 acc[2][2][5];
#pragma unroll
  for (int a = 0; a < 2; ++a)
#pragma unroll
    for (int m = 0; m < 2; ++m)
#pragma unroll
      for (int n = 0; n < 5; ++n) acc[a][m][n] = (f32x4){};
#pragma unroll
  for (int i = 0; i < DIST; ++i) ISSUE(i, i);
#define ROUND(r, slot, AB, YOUNGER, DOISSUE) do { \
    wait_round<WM, YOUNGER>(); asm volatile("" : "+v"(rg.x[slot][0]), "+v"(rg.x[slot][1])); \
    asm volatile("s_barrier" ::: "memory"); SBAR(); \
    s16x4 wl[5], wh[5]; \
    _Pragma("unroll") for (int n = 0; n < 5; ++n) { wl[n] = lds_rdtr(wrd + (slot) * SLOTB + n * 32); wh[n] = lds_rdtr(wrd + (slot) * SLOTB + n * 32 + 8 * 160); } \
    SBAR(); \
    if (DOISSUE) ISSUE((r) + DIST, ((slot) + DIST) % NSLOT); \
    SBAR(); \
    _Pragma("unroll") for (int n = 0; n < 5; ++n) { const bf16x8 w = __builtin_shufflevector(wl[n], wh[n], 0, 1, 2, 3, 4, 5, 6, 7); \
      acc[AB][0][n] = MFMA16(w, rg.x[slot][0], acc[AB][0][n]); acc[AB][1][n] = MFMA16(w, rg.x[slot][1], acc[AB][1][n]); } \
    asm volatile("s_waitcnt lgkmcnt(0)" ::: "memory"); SBAR(); } while (0)
  for (int r = 0; r < (NSLOT == 8 ? RA - 8 : RA); r += NSLOT) {
    ROUND(r, 0, 0, DIST - 1, true); ROUND(r + 1, 1, 0, DIST - 1, true); ROUND(r + 2, 2, 0, DIST - 1, true); ROUND(r + 3, 3, 0, DIST - 1, true);
    if constexpr (NSLOT == 8) { ROUND(r + 4, 4 % NSLOT, 0, DIST - 1, true); ROUND(r + 5, 5 % NSLOT, 0, DIST - 1, true); ROUND(r + 6, 6 % NSLOT, 0, DIST - 1, true); ROUND(r + 7, 7 % NSLOT, 0, DIST - 1, true); }
  }
  if constexpr (NSLOT == 8) {
    ROUND(24, 0, 0, 6, true); ROUND(25, 1 % NSLOT, 0, 6, true); ROUND(26, 2 % NSLOT, 0, 6, true); ROUND(27, 3 % NSLOT, 0, 6, true); ROUND(28, 4 % NSLOT, 0, 6, true); ROUND(29, 5 % NSLOT, 0, 6, true);
    ROUND(30, 6 % NSLOT, 0, 6, false); ROUND(31, 7 % NSLOT, 0, 5, false);
  }
  if constexpr (NSLOT == 4) { ROUND(RA, 0, 1, 2, true); ROUND(RA + 1, 1, 1, 2, true); ROUND(RA + 2, 2, 1, 2, false); ROUND(RA + 3, 3, 1, 1, false); ROUND(RA + 4, 0, 1, 0, false); }
  else { ROUND(RA, 0, 1, 4, false); ROUND(RA + 1, 1, 1, 3, false); ROUND(RA + 2, 2, 1, 2, false); ROUND(RA + 3, 3, 1, 1, false); ROUND(RA + 4, 4 % NSLOT, 1, 0, false); }
#undef ROUND
#undef ISSUE
  asm volatile("s_barrier" ::: "memory");
  LAS char* red = (LAS char*)lds + WM * 40960 + lane * 16;
#pragma unroll
  for (int a = 0; a < 2; ++a) {
#pragma unroll
    for (int t = 0; t < 10; ++t) { const int own = t & 3; if (wk != own) { const int rank = (wk - own - 1) & 3; *(LAS f32x4*)(red + (t * 3 + rank) * 1024) = acc[a][t / 5][t % 5]; } }
    asm volatile("s_waitcnt lgkmcnt(0)\n\ts_barrier" ::: "memory");
#pragma unroll
    for (int t = 0; t < 10; ++t) if (wk == (t & 3)) {
      f32x4 s = acc[a][t / 5][t % 5];
#pragma unroll
      for (int k = 0; k < 3; ++k) s += *(const LAS f32x4*)(red + (t * 3 + k) * 1024);
      const int row = pm * 64 + WM * 32 + (t / 5) * 16 + (lane & 15), col = a * 640 + pn * 80 + (t % 5) * 16 + 4 * (lane >> 4);
      *(u32x2*)(out + (size_t)row * 1280 + col) = (u32x2){cvtpk(s[0], s[1]), cvtpk(s[2], s[3])};
    }
    asm volatile("s_waitcnt lgkmcnt(0)\n\ts_barrier" ::: "memory");
  }
}

__global__ __launch_bounds__(512, 2) void outp_kernel(const u16* __restrict__ Cf, const u16* __restrict__ Of, const u16* __restrict__ Wo, const u16* __restrict__ WoB, u16* __restrict__ out) {
  extern __shared__ __attribute__((aligned(16))) char smem[];
  const LAS char* lds = (const LAS char*)smem;
  const int tid = threadIdx.x, lane = tid & 63, wid = (int)rfl(tid >> 6), wm = wid & 1, wk = wid >> 1;
  const int x = blockIdx.x & 7, j = blockIdx.x >> 3, pm = 8 * (x >> 1) + (j >> 2), pn = 4 * (x & 1) + (j & 3);
  if (wm == 0) body<0>(Cf, Of, Wo, WoB, out, lds, pm, pn, wk, lane);
  else body<1>(Cf, Of, Wo, WoB, out, lds, pm, pn, wk, lane);
}
}

extern "C" void kernel_launch(void* const* d_in, const int* in_sizes, int n_in, void* d_out, int out_size, void* d_ws, size_t ws_size, hipStream_t stream) {
  const u16* Ax = (const u16*)d_in[0]; const u16* ckv = (const u16*)d_in[1]; const u16* wq = (const u16*)d_in[2]; const u16* wuk = (const u16*)d_in[3];
  const u16* wuv = (const u16*)d_in[4]; const u16* wo = (const u16*)d_in[5]; const u16* Bx = (const u16*)d_in[6]; const u16* wqkv = (const u16*)d_in[7];
  const u16* wphi = (const u16*)d_in[8]; const u16* Bwo = (const u16*)d_in[9]; u16* out = (u16*)d_out;
  char* ws = (char*)d_ws;
  u16* Qf = (u16*)(ws); u16* Kf = (u16*)(ws + (16u << 20)); u16* Vf = (u16*)(ws + (32u << 20)); u16* Cf = (u16*)(ws + (48u << 20));
  u16* Braw = (u16*)(ws + (64u << 20)); u16* Of = (u16*)(ws + (72u << 20)); float* part = (float*)(ws + (76u << 20));
  (void)hipFuncSetAttribute((const void*)proj::proj_kernel, hipFuncAttributeMaxDynamicSharedMemorySize, proj::LDS_BYTES);
  (void)hipFuncSetAttribute((const void*)attn::attn_kernel, hipFuncAttributeMaxDynamicSharedMemorySize, attn::LDS_BYTES);
  (void)hipFuncSetAttribute((const void*)outp::outp_kernel, hipFuncAttributeMaxDynamicSharedMemorySize, outp::LDS_BYTES);
  proj::proj_kernel<<<256, 512, proj::LDS_BYTES, stream>>>(Ax, ckv, wq, wuk, wuv, Bx, wqkv, Qf, Kf, Vf, Braw, nullptr);
  bs::b_kv<<<dim3(32, 8), 64, 0, stream>>>(Braw, wphi, part);
  bs::b_o<<<dim3(32, 32), 64, 0, stream>>>(Braw, wphi, part, Of);
  attn::attn_kernel<<<256, 512, attn::LDS_BYTES, stream>>>(Qf, Kf, Vf, Cf, nullptr);
  outp::outp_kernel<<<256, 512, outp::LDS_BYTES, stream>>>(Cf, Of, wo, Bwo, out);
}
```

```cpp
#include <hip/hip_runtime.h>
#include <stdint.h>
typedef unsigned short u16;
typedef short bf16x8 __attribute__((ext_vector_type(8)));
typedef short s16x4 __attribute__((ext_vector_type(4)));
typedef float f32x4 __attribute__((ext_vector_type(4)));
typedef float f32x2 __attribute__((ext_vector_type(2)));
typedef __bf16 bf16x2_t __attribute__((ext_vector_type(2)));
typedef unsigned u32x4 __attribute__((ext_vector_type(4)));
typedef unsigned u32x2 __attribute__((ext_vector_type(2)));
typedef __amdgpu_buffer_rsrc_t rsrc_t;
#define LAS __attribute__((address_space(3)))
#define DI __device__ __forceinline__
#define SBAR() __builtin_amdgcn_sched_barrier(0)
#define PIN(x) asm volatile("" : "+v"(x))
#define MFMA16(a, b, c) __builtin_amdgcn_mfma_f32_16x16x32_bf16(a, b, c, 0, 0, 0)
DI void mfma_acc(f32x4& acc, const bf16x8& a, const bf16x8& b) { asm volatile("v_mfma_f32_16x16x32_bf16 %0, %1, %2, %0" : "+v"(acc) : "v"(a), "v"(b)); }
DI void mfma_init(f32x4& d, const bf16x8& a, const bf16x8& b, const f32x4& c) { asm volatile("v_mfma_f32_16x16x32_bf16 %0, %1, %2, %3" : "=&v"(d) : "v"(a), "v"(b), "v"(c)); }
DI void mfma_zero(f32x4& d, const bf16x8& a, const bf16x8& b) { asm volatile("v_mfma_f32_16x16x32_bf16 %0, %1, %2, 0" : "=&v"(d) : "v"(a), "v"(b)); }
#define MFMA_SETTLE() do { SBAR(); asm volatile("s_nop 7\n\ts_nop 1" ::: "memory"); SBAR(); } while (0)
DI unsigned rfl(unsigned x) { return __builtin_amdgcn_readfirstlane(x); }
DI unsigned cvtpk(float lo, float hi) { f32x2 v = {lo, hi}; bf16x2_t b = __builtin_convertvector(v, bf16x2_t); return __builtin_bit_cast(unsigned, b); }
DI float bf_lo(unsigned w) { return __uint_as_float(w << 16); }
DI float bf_hi(unsigned w) { return __uint_as_float(w & 0xffff0000u); }
DI rsrc_t mk_rsrc(const void* p, unsigned bytes) { return __builtin_amdgcn_make_buffer_rsrc((void*)p, (short)0, (int)bytes, 0x00020000); }
DI bf16x8 lds_rd128(const LAS char* p) { return *(const LAS bf16x8*)p; }
typedef short v4i16_t __attribute__((ext_vector_type(4)));
DI s16x4 lds_rdtr(const LAS char* p) { return __builtin_bit_cast(s16x4, __builtin_amdgcn_ds_read_tr16_b64_v4i16((LAS v4i16_t*)p)); }
DI float max3f(float a, float b, float c) { float r; asm("v_max3_f32 %0, %1, %2, %3" : "=v"(r) : "v"(a), "v"(b), "v"(c)); return r; }
DI float fadd1(float a, float b) { float r; asm("v_add_f32_e32 %0, %1, %2" : "=v"(r) : "v"(a), "v"(b)); return r; }
DI void set_m0(unsigned a) { asm volatile("s_mov_b32 m0, %0\n\ts_nop 0" ::"s"(a) : "memory"); }
template <int OFF> DI void dma16m(unsigned voff, rsrc_t srd, unsigned soff) {
  asm volatile("buffer_load_dwordx4 %0, %1, %2 offen offset:%c3 lds" ::"v"(voff), "s"(srd), "s"(soff), "i"(OFF) : "memory");
}
DI void dma16(unsigned ldsaddr, unsigned voff, rsrc_t srd, unsigned soff) {
  asm volatile("s_mov_b32 m0, %0\n\ts_nop 0\n\tbuffer_load_dwordx4 %1, %2, %3 offen lds" ::"s"(ldsaddr), "v"(voff), "s"(srd), "s"(soff) : "memory");
}
typedef unsigned srd_t __attribute__((ext_vector_type(4)));
DI srd_t mk_srd(const void* p, unsigned bytes) { const unsigned long long a = (unsigned long long)p; return (srd_t){(unsigned)a, (unsigned)(a >> 32) & 0xffffu, bytes, 0x00020000u}; }
DI void dma16s(unsigned ldsaddr, unsigned voff, srd_t srd, unsigned soff) {
  asm volatile("s_mov_b32 m0, %0\n\ts_nop 0\n\tbuffer_load_dwordx4 %1, %2, %3 offen lds" ::"s"(ldsaddr), "v"(voff), "s"(srd), "s"(soff) : "memory");
}
DI void touch_lds(unsigned ldsaddr, unsigned voff, rsrc_t srd, unsigned soff) {
  asm volatile("s_mov_b32 m0, %0\n\ts_nop 0\n\tbuffer_load_dword %1, %2, %3 offen lds" ::"s"(ldsaddr), "v"(voff), "s"(srd), "s"(soff) : "memory");
}
template <int AUX> DI void st128(const u32x4& w, rsrc_t r, unsigned voff) { asm volatile("" : "+v"(voff)); __builtin_amdgcn_raw_buffer_store_b128(w, r, voff, 0, AUX); }
template <int AUX> DI void st64(const u32x2& w, rsrc_t r, unsigned voff) { asm volatile("" : "+v"(voff)); __builtin_amdgcn_raw_buffer_store_b64(w, r, voff, 0, AUX); }
DI float xrow_max(float x) {
  auto a = __builtin_amdgcn_permlane32_swap(__float_as_uint(x), __float_as_uint(x), false, false); x = fmaxf(__uint_as_float(a[0]), __uint_as_float(a[1]));
  auto b = __builtin_amdgcn_permlane16_swap(__float_as_uint(x), __float_as_uint(x), false, false); return fmaxf(__uint_as_float(b[0]), __uint_as_float(b[1]));
}
DI float xrow_sum(float x) {
  auto a = __builtin_amdgcn_permlane32_swap(__float_as_uint(x), __float_as_uint(x), false, false); x = __uint_as_float(a[0]) + __uint_as_float(a[1]);
  auto b = __builtin_amdgcn_permlane16_swap(__float_as_uint(x), __float_as_uint(x), false, false); return __uint_as_float(b[0]) + __uint_as_float(b[1]);
}
DI u32x4 pair_swap(unsigned x0, unsigned x1, unsigned y0, unsigned y1) {
  auto a = __builtin_amdgcn_permlane16_swap(x0, y0, false, false);
  auto b = __builtin_amdgcn_permlane16_swap(x1, y1, false, false);
  return (u32x4){a[0], b[0], a[1], b[1]};
}

namespace proj {
constexpr int LDS_X = 0, LDS_W = 65536, LDS_BYTES = 163840;
constexpr float QSCALE = 0.08838834764831845f * 1.4426950408889634f;

struct Src {
  rsrc_t X, W; unsigned vX[4], vW[2]; unsigned sX, sW, wstep, rowX8;
  unsigned rowW2;
};
struct Frag { bf16x8 x[8]; s16x4 wl[2][4], wh[2][4]; };
#define WF(ks, n) __builtin_shufflevector(fr.wl[ks][n], fr.wh[ks][n], 0, 1, 2, 3, 4, 5, 6, 7)
struct Adr { const LAS char* x[2][2]; const LAS char* w[4]; };

enum { U_Q = 0, U_K = 1, U_V = 2, U_B = 3 };
struct Unit { int kind, pm, pn; };

struct Ptrs { const u16 *Ax, *ckv, *Bx, *wq, *wuk, *wuv, *wqkv; };
DI int fresh_lane() { int l; asm volatile("v_mbcnt_lo_u32_b32 %0, -1, 0\n\tv_mbcnt_hi_u32_b32 %0, -1, %0" : "=v"(l)); return l; }
DI void setup_src(Src& s, const Unit& u, const Ptrs& P, int wid) {
  const int lane = fresh_lane();
  const u16 *Ax = P.Ax, *ckv = P.ckv, *Bx = P.Bx, *wq = P.wq, *wuk = P.wuk, *wuv = P.wuv, *wqkv = P.wqkv;
  const int ldx = (u.kind == U_K || u.kind == U_V) ? 512 : 640;
  const u16* X = u.kind == U_Q ? Ax : (u.kind == U_B ? Bx : ckv);
  s.X = mk_rsrc(X, 2048u * (unsigned)ldx * 2u);
  const int r = lane >> 3, f0 = ((r >> 1) & 1) | (((r >> 2) & 1) << 1);
#pragma unroll
  for (int i = 0; i < 4; ++i) s.vX[i] = (unsigned)(r * ldx * 2) + (unsigned)((((lane & 7) ^ f0 ^ (((i >> 1) & 1) << 1) ^ ((i & 1) << 2)) & 7) * 16);
  s.rowX8 = (unsigned)(8 * ldx * 2);
  s.sX = (unsigned)((u.pm * 256 + wid * 32) * ldx * 2);
  if (u.kind == U_B) {
    s.W = mk_rsrc(wqkv, 640u * 1920u * 2u);
#pragma unroll
    for (int p = 0; p < 2; ++p) { const int sg = ((2 * p + (lane >> 5)) & 3) | ((wid & 1) << 2); const int ch = (lane & 31) ^ (sg << 1);
      const int col = ch < 10 ? u.pn * 160 + ch * 16 : (ch < 20 ? 1280 + u.pn * 160 + (ch - 10) * 16 : (ch < 30 ? 2560 + u.pn * 160 + (ch - 20) * 16 : u.pn * 160));
      s.vW[p] = (unsigned)((lane >> 5) * 3840 + col); }
    s.rowW2 = 2u * 3840u; s.wstep = 64u * 3840u; s.sW = (unsigned)(wid * 8 * 3840);
  } else {
    const u16* W = u.kind == U_Q ? wq : (u.kind == U_K ? wuk : wuv);
    s.W = mk_rsrc(W, (u.kind == U_Q ? 640u : 512u) * 4096u * 2u);
#pragma unroll
    for (int p = 0; p < 2; ++p) { const int sg = ((2 * p + (lane >> 5)) & 3) | ((wid & 1) << 2); s.vW[p] = (unsigned)((lane >> 5) * 8192 + (((lane & 31) ^ (sg << 1)) * 16)); }
    s.rowW2 = 2u * 8192u; s.wstep = 64u * 8192u; s.sW = (unsigned)(wid * 8 * 8192 + u.pn * 512);
  }
}
template <int J> DI void dma_piece(const Src& s, unsigned ldsw, int slot) {
  if constexpr (J < 4) dma16(ldsw + LDS_X + slot * 32768 + J * 1024, s.vX[J], s.X, s.sX + J * s.rowX8);
  else dma16(ldsw + LDS_W + slot * 32768 + (J - 4) * 1024, s.vW[(J - 4) & 1], s.W, s.sW + (J - 4) * s.rowW2);
}
DI void src_next(Src& s) { s.sX += 128u; s.sW += s.wstep; }

DI void setup_adr(Adr& a, const LAS char* lds, bool kappa, int wr, int wc) {
  const int lane = fresh_lane();
  const int c = lane & 15, g = lane >> 4;
#pragma unroll
  for (int par = 0; par < 2; ++par) {
    const int row = kappa ? 8 * (c >> 2) + 4 * par + (c & 3) : 16 * par + c;
    const int f = ((row >> 1) & 1) | ((((row >> 2) ^ (row >> 4)) & 1) << 1) | (((row >> 3) & 1) << 2);
#pragma unroll
    for (int ks = 0; ks < 2; ++ks) a.x[par][ks] = lds + LDS_X + wr * 16384 + row * 128 + (((4 * ks + g) ^ f) * 16);
  }
  const int q = (lane & 15) >> 2, p = lane & 3, sg = q | ((g & 1) << 2);
#pragma unroll
  for (int n = 0; n < 4; ++n) a.w[n] = lds + LDS_W + (8 * g + q) * 512 + (((4 * wc + n) ^ sg) * 32) + 8 * p;
}
#define RDX(SL, f) (fr.x[(f) & 7] = lds_rd128(ad.x[(f) & 1][((f) >> 3) & 1] + (SL) * 32768 + (((f) & 7) >> 1) * 4096))
#define RDW(SL, ks, n, h) do { if (h) fr.wh[ks][n] = lds_rdtr(ad.w[n] + (SL) * 32768 + (ks) * 16384 + 2048); else fr.wl[ks][n] = lds_rdtr(ad.w[n] + (SL) * 32768 + (ks) * 16384); } while (0)

template <int SL, bool FIRST, bool SWAP>
DI void ktile(f32x4 (&acc)[8][4], Frag& fr, const Adr& ad, Src& src, const Unit& nu, const Ptrs& P, int wid, bool switch_src, bool do_dma, unsigned ldsw) {
#define MM(f, n) do { \
    if (FIRST && (f) < 8) { if (SWAP) mfma_zero(acc[(f) & 7][n], fr.x[(f) & 7], WF(0, n)); else mfma_zero(acc[(f) & 7][n], WF(0, n), fr.x[(f) & 7]); } \
    else { if (SWAP) mfma_acc(acc[(f) & 7][n], fr.x[(f) & 7], WF((f) >> 3, n)); else mfma_acc(acc[(f) & 7][n], WF((f) >> 3, n), fr.x[(f) & 7]); } SBAR(); } while (0)
#define GRP(f) do { \
    if ((f) == 9) { asm volatile("s_waitcnt vmcnt(0) lgkmcnt(0)\n\ts_barrier" ::: "memory"); SBAR(); if (switch_src) setup_src(src, nu, P, wid); } \
    MM(f, 0); \
    if ((f) < 4) { RDW(SL, 1, (f) & 3, 0); } else if ((f) >= 9 && (f) < 13) { RDW(SL ^ 1, 0, ((f) - 9) & 3, 0); } \
    SBAR(); \
    MM(f, 1); \
    if ((f) < 4) { RDW(SL, 1, (f) & 3, 1); } else if ((f) >= 9 && (f) < 13) { RDW(SL ^ 1, 0, ((f) - 9) & 3, 1); } \
    SBAR(); \
    MM(f, 2); \
    if (do_dma) { if ((f) == 9) dma_piece<0>(src, ldsw, SL); if ((f) == 10) dma_piece<4>(src, ldsw, SL); if ((f) == 11) dma_piece<1>(src, ldsw, SL); if ((f) == 12) dma_piece<5>(src, ldsw, SL); \
                  if ((f) == 13) dma_piece<2>(src, ldsw, SL); if ((f) == 14) dma_piece<6>(src, ldsw, SL); if ((f) == 15) { dma_piece<3>(src, ldsw, SL); } } \
    SBAR(); \
    MM(f, 3); \
    if ((f) == 15 && do_dma) { dma_piece<7>(src, ldsw, SL); src_next(src); } \
    if ((f) < 8) RDX(SL, (f) + 8); else if ((f) == 9) { RDX(SL ^ 1, 0); SBAR(); RDX(SL ^ 1, 1); } else if ((f) > 9) RDX(SL ^ 1, (f) - 8); \
    SBAR(); } while (0)
  GRP(0); GRP(1); GRP(2); GRP(3); GRP(4); GRP(5); GRP(6); GRP(7); GRP(8); GRP(9); GRP(10); GRP(11); GRP(12); GRP(13); GRP(14); GRP(15);
#undef GRP
#undef MM
}

template <bool SWAP>
DI void run_unit(f32x4 (&acc)[8][4], const Adr& ad, Src& src, const Unit& nu, const Ptrs& P, int wid, bool has_next, int nt, unsigned ldsw) {
  Frag fr;
  if (SWAP) asm volatile("; unit: tokens on rows" ::: "memory"); else asm volatile("; unit: weights on rows" ::: "memory");
  RDW(0, 0, 0, 0); RDW(0, 0, 0, 1); RDX(0, 0); RDW(0, 0, 1, 0); RDW(0, 0, 1, 1); RDW(0, 0, 2, 0); RDW(0, 0, 2, 1); RDW(0, 0, 3, 0); RDW(0, 0, 3, 1);
  RDX(0, 1); RDX(0, 2); RDX(0, 3); RDX(0, 4); RDX(0, 5); RDX(0, 6); RDX(0, 7);
  SBAR();
  ktile<0, true, SWAP>(acc, fr, ad, src, nu, P, wid, false, true, ldsw);
  ktile<1, false, SWAP>(acc, fr, ad, src, nu, P, wid, false, true, ldsw);
  for (int t = 2; t < nt - 2; t += 2) {
    ktile<0, false, SWAP>(acc, fr, ad, src, nu, P, wid, false, true, ldsw);
    ktile<1, false, SWAP>(acc, fr, ad, src, nu, P, wid, false, true, ldsw);
  }
  ktile<0, false, SWAP>(acc, fr, ad, src, nu, P, wid, has_next, has_next, ldsw);
  ktile<1, false, SWAP>(acc, fr, ad, src, nu, P, wid, false, has_next, ldsw);
  MFMA_SETTLE();
}

#define ST_AUX 0
DI void st16(const u32x4& w, rsrc_t r, unsigned voff, unsigned soff) { __builtin_amdgcn_raw_buffer_store_b128(w, r, voff + soff, 0, ST_AUX); }
DI void epi_qk(const f32x4 (&acc)[8][4], const Unit& u, u16* __restrict__ dst, float scale, int wr, int wc) {
  const int lane = fresh_lane();
  const int g = lane >> 4; const unsigned voff = (unsigned)((lane & 15) + 16 * (((g & 1) << 1) | (g >> 1))) * 16u;
  const rsrc_t r = mk_rsrc(dst, 2048u * 4096u * 2u);
  const unsigned base = (unsigned)(((u.pn * 2 + (wc >> 1)) * 128 + u.pm * 16 + wr * 8) * 4 + (wc & 1) * 2) * 1024u;
#pragma unroll
  for (int m = 0; m < 8; ++m)
#pragma unroll
    for (int t = 0; t < 2; ++t) {
      const f32x4 a = acc[m][2 * t] * scale, b = acc[m][2 * t + 1] * scale;
      st16(pair_swap(cvtpk(a[0], a[1]), cvtpk(a[2], a[3]), cvtpk(b[0], b[1]), cvtpk(b[2], b[3])), r, voff + t * 1024, base + m * 4096);
    }
}
DI void epi_v(const f32x4 (&acc)[8][4], const Unit& u, u16* __restrict__ Vf, int wr, int wc) {
  const unsigned voff = (unsigned)fresh_lane() * 16u;
  const rsrc_t r = mk_rsrc(Vf, 2048u * 4096u * 2u);
  const unsigned base = (unsigned)(((u.pn * 2 + (wc >> 1)) * 64 + u.pm * 8 + wr * 4) * 8 + (wc & 1) * 4) * 1024u;
#pragma unroll
  for (int mm = 0; mm < 4; ++mm)
#pragma unroll
    for (int n = 0; n < 4; ++n) {
      const f32x4 a = acc[2 * mm][n], b = acc[2 * mm + 1][n];
      st16((u32x4){cvtpk(a[0], a[1]), cvtpk(a[2], a[3]), cvtpk(b[0], b[1]), cvtpk(b[2], b[3])}, r, voff + n * 1024, base + mm * 8192);
    }
}
constexpr int T_PITCH = 528, PH_OFF = 256 * T_PITCH, PH_PITCH = 96, PH_WAVE = 32 * PH_PITCH;
static_assert(PH_OFF + 8 * PH_WAVE + 6 * 1024 <= LDS_BYTES + 6 * 1024 && PH_OFF + 8 * PH_WAVE <= LDS_BYTES, "LDS map");
#define BSTAMP(i)
DI void epi_b(const f32x4 (&acc)[8][4], const Unit& u, const LAS char* lds_c, const u16* __restrict__ wphi, u16* __restrict__ PhiQ, float* __restrict__ Part, int wid, unsigned long long* bts) {
  LAS char* lds = (LAS char*)lds_c;
  const int lane = fresh_lane(), c = lane & 15, g = lane >> 4, gp = ((g & 1) << 1) | (g >> 1), wr = wid >> 2, wc = wid & 3;
  const int hh = wid & 3, half = wid >> 2, head = u.pn * 4 + hh;
  asm volatile("s_waitcnt lgkmcnt(0)\n\ts_barrier" ::: "memory");
#pragma unroll
  for (int m = 0; m < 8; ++m)
#pragma unroll
    for (int t = 0; t < 2; ++t) { const f32x4 a = acc[m][2 * t], b = acc[m][2 * t + 1];
      *(LAS u32x4*)(lds + (wr * 128 + m * 16 + c) * T_PITCH + (wc * 64 + t * 32 + gp * 8) * 2) = pair_swap(cvtpk(a[0], a[1]), cvtpk(a[2], a[3]), cvtpk(b[0], b[1]), cvtpk(b[2], b[3])); }
  SBAR();
  u16 wraw[16];
#pragma unroll
  for (int j = 0; j < 8; ++j) { const int d = 8 * g + j, dd = d < 20 ? d : 0; wraw[j] = wphi[head * 400 + dd * 20 + c]; wraw[8 + j] = wphi[head * 400 + dd * 20 + 16 + (c & 3)]; }
  bf16x8 w0, w1;
#pragma unroll
  for (int j = 0; j < 8; ++j) { const int d = 8 * g + j; w0[j] = d < 20 ? (short)wraw[j] : (short)0; w1[j] = (d < 20 && c < 4) ? (short)wraw[8 + j] : (short)0; }
  asm volatile("s_waitcnt lgkmcnt(0)\n\ts_barrier" ::: "memory");
  BSTAMP(0);
  LAS char* ph = lds + PH_OFF + wid * PH_WAVE;
  const rsrc_t rq = mk_rsrc(PhiQ, 32u * 128u * 1280u);
  const unsigned vq0 = (unsigned)lane * 16u, vq1 = g == 0 ? 1024u + (unsigned)c * 16u : 0x80000000u;
  f32x4 kv[3][2];
#pragma unroll
  for (int i = 0; i < 3; ++i) { kv[i][0] = (f32x4){}; kv[i][1] = (f32x4){}; }
  const int q4 = c >> 2, p4 = c & 3;
  const unsigned mlo = g == 3 ? 0u : 0xffffffffu, mhi = g >= 2 ? 0u : 0xffffffffu;
  const float NEG = -__builtin_inff(), POS = __builtin_inff();
  constexpr float L2E = 1.4426950408889634f;
  for (int step = 0; step < 4; ++step) {
    const int r0 = half * 128 + step * 32;
#pragma unroll
    for (int b2 = 0; b2 < 2; ++b2) {
    f32x4 a[2], b[2];
#pragma unroll
    for (int i = 0; i < 2; ++i) { const LAS char* xr = lds + (r0 + b2 * 16 + c) * T_PITCH + (i * 80 + hh * 20 + 8 * g) * 2;
      u32x2 lo = *(const LAS u32x2*)xr, hi = *(const LAS u32x2*)(xr + 8); lo &= mlo; hi &= mhi;
      const bf16x8 x = __builtin_bit_cast(bf16x8, (u32x4){lo[0], lo[1], hi[0], hi[1]});
      a[i] = MFMA16(w0, x, ((f32x4){})); b[i] = MFMA16(w1, x, ((f32x4){})); }
    float mx[2], mn[2];
#pragma unroll
    for (int i = 0; i < 2; ++i) {
      const float bx = g == 0 ? fmaxf(fmaxf(b[i][0], b[i][1]), fmaxf(b[i][2], b[i][3])) : NEG, bn = g == 0 ? fminf(fminf(b[i][0], b[i][1]), fminf(b[i][2], b[i][3])) : POS;
      mx[i] = fmaxf(fmaxf(fmaxf(a[i][0], a[i][1]), fmaxf(a[i][2], a[i][3])), bx); mn[i] = -fminf(fminf(fminf(a[i][0], a[i][1]), fminf(a[i][2], a[i][3])), bn); }
#pragma unroll
    for (int i = 0; i < 2; ++i) { mx[i] = xrow_max(mx[i]) * L2E; mn[i] = xrow_max(mn[i]) * L2E; }
    f32x4 p0[2], p1[2], m0[2], m1[2]; float sp[2], sm[2];
#pragma unroll
    for (int i = 0; i < 2; ++i) { const float mxb = g == 0 ? mx[i] : POS, mnb = g == 0 ? mn[i] : POS;
#pragma unroll
      for (int r = 0; r < 4; ++r) { p0[i][r] = __builtin_amdgcn_exp2f(fmaf(a[i][r], L2E, -mx[i])); m0[i][r] = __builtin_amdgcn_exp2f(fmaf(a[i][r], -L2E, -mn[i]));
                                    p1[i][r] = __builtin_amdgcn_exp2f(fmaf(b[i][r], L2E, -mxb)); m1[i][r] = __builtin_amdgcn_exp2f(fmaf(b[i][r], -L2E, -mnb)); }
      sp[i] = ((p0[i][0] + p0[i][1]) + (p0[i][2] + p0[i][3])) + ((p1[i][0] + p1[i][1]) + (p1[i][2] + p1[i][3]));
      sm[i] = ((m0[i][0] + m0[i][1]) + (m0[i][2] + m0[i][3])) + ((m1[i][0] + m1[i][1]) + (m1[i][2] + m1[i][3])); }
#pragma unroll
    for (int i = 0; i < 2; ++i) { sp[i] = __builtin_amdgcn_rcpf(xrow_sum(sp[i])); sm[i] = __builtin_amdgcn_rcpf(xrow_sum(sm[i])); }
#pragma unroll
    for (int i = 0; i < 2; ++i) {
      const u32x4 f0 = {cvtpk(p0[i][0] * sp[i], p0[i][1] * sp[i]), cvtpk(p0[i][2] * sp[i], p0[i][3] * sp[i]), cvtpk(m0[i][0] * sm[i], m0[i][1] * sm[i]), cvtpk(m0[i][2] * sm[i], m0[i][3] * sm[i])};
      const u32x4 f1 = {cvtpk(p1[i][0] * sp[i], p1[i][1] * sp[i]), cvtpk(p1[i][2] * sp[i], p1[i][3] * sp[i]), cvtpk(m1[i][0] * sm[i], m1[i][1] * sm[i]), cvtpk(m1[i][2] * sm[i], m1[i][3] * sm[i])};
      if (i == 0) {
        const unsigned so = (unsigned)(head * 128 + u.pm * 16 + (r0 >> 4) + b2) * 1280u;
        st16(f0, rq, vq0, so); st16(f1, rq, vq1, so);
      } else {
        LAS char* pr = ph + (b2 * 16 + c) * PH_PITCH;
        *(LAS u32x2*)(pr + 8 * g) = (u32x2){f0[0], f0[1]};
        *(LAS u32x2*)(pr + 32 + 8 * g) = (u32x2){f0[2], f0[3]};
        *(LAS u32x4*)(pr + (g == 0 ? 64 : 80)) = f1;
      }
    }
    SBAR();
    }
    asm volatile("s_waitcnt lgkmcnt(0)" ::: "memory");
    bf16x8 vb[2];
#pragma unroll
    for (int db = 0; db < 2; ++db) { const LAS char* av = lds + (r0 + 8 * g + q4) * T_PITCH + (160 + hh * 20 + 16 * db + 4 * p4) * 2;
      vb[db] = __builtin_shufflevector(lds_rdtr(av), lds_rdtr(av + 4 * T_PITCH), 0, 1, 2, 3, 4, 5, 6, 7); }
    if (c == 4) vb[1] = (bf16x8){0x3F80, 0x3F80, 0x3F80, 0x3F80, 0x3F80, 0x3F80, 0x3F80, 0x3F80};
#pragma unroll
    for (int ab = 0; ab < 3; ++ab) { const LAS char* ap = ph + (8 * g + q4) * PH_PITCH + (16 * ab + 4 * p4) * 2;
      const bf16x8 pa = __builtin_shufflevector(lds_rdtr(ap), lds_rdtr(ap + 4 * PH_PITCH), 0, 1, 2, 3, 4, 5, 6, 7);
      kv[ab][0] = MFMA16(pa, vb[0], kv[ab][0]); kv[ab][1] = MFMA16(pa, vb[1], kv[ab][1]); }
    asm volatile("s_waitcnt lgkmcnt(0)" ::: "memory");
  }
  BSTAMP(1);
  asm volatile("s_barrier" ::: "memory");
  LAS char* ex = lds + PH_OFF + hh * 6144 + lane * 16;
  if (half == 1) {
#pragma unroll
    for (int i = 0; i < 6; ++i) *(LAS f32x4*)(ex + i * 1024) = kv[i >> 1][i & 1]; }
  asm volatile("s_waitcnt lgkmcnt(0)\n\ts_barrier" ::: "memory");
  if (half == 0) {
#pragma unroll
    for (int i = 0; i < 6; ++i) ((f32x4*)Part)[(((size_t)u.pm * 32 + head) * 6 + i) * 64 + lane] = kv[i >> 1][i & 1] + *(const LAS f32x4*)(ex + i * 1024); }
}
DI void relayout_w(const u16* __restrict__ wo, const u16* __restrict__ Bwo, u16* __restrict__ WfA, u16* __restrict__ WfB, const LAS char* lds, int wid, int gw, int nw) {
  const int lane = fresh_lane(), g = lane >> 4, q4 = (lane & 15) >> 2, p4 = lane & 3;
  const unsigned ldsw = (unsigned)(uintptr_t)lds + (unsigned)wid * 16384u;
  const rsrc_t rA = mk_rsrc(wo, 4096u * 640u * 2u), rB = mk_rsrc(Bwo, 640u * 640u * 2u);
  const unsigned voff = (unsigned)((lane >> 1) * 1280 + (lane & 1) * 16);
  const LAS char* rd = lds + wid * 16384 + (8 * g + q4) * 32 + 8 * p4;
  for (int it = gw; it < 1480; it += 3 * nw) {
#pragma unroll
    for (int e = 0; e < 3; ++e) { const int x = it + e * nw; const int ks = x / 10, ng = x - ks * 10; const bool isA = ks < 128; const rsrc_t r = isA ? rA : rB;
      const unsigned so = x < 1480 ? (unsigned)((isA ? ks : ks - 128) * 32 * 1280 + ng * 128) : 0u;
#pragma unroll
      for (int n = 0; n < 4; ++n) dma16(ldsw + (unsigned)(e * 4 + n) * 1024u, voff, r, so + n * 32u); }
    asm volatile("s_waitcnt vmcnt(0)" ::: "memory");
#pragma unroll
    for (int e = 0; e < 3; ++e) { const int x = it + e * nw; const int ks = x / 10, ng = x - ks * 10;
#pragma unroll
      for (int n = 0; n < 4; ++n) { const LAS char* a = rd + (e * 4 + n) * 1024;
        const bf16x8 w = __builtin_shufflevector(lds_rdtr(a), lds_rdtr(a + 4 * 32), 0, 1, 2, 3, 4, 5, 6, 7);
        if (x < 1480) { bf16x8* dst = ks < 128 ? (bf16x8*)WfA + (size_t)ks * 40 * 64 : (bf16x8*)WfB + (size_t)(ks - 128) * 40 * 64; dst[(ng * 4 + n) * 64 + lane] = w; } } }
    asm volatile("s_waitcnt lgkmcnt(0)" ::: "memory");
  }
}

__global__ __launch_bounds__(512, 2) void proj_kernel(const u16* __restrict__ Ax, const u16* __restrict__ ckv, const u16* __restrict__ wq, const u16* __restrict__ wuk, const u16* __restrict__ wuv,
                                                      const u16* __restrict__ Bx, const u16* __restrict__ wqkv, u16* __restrict__ Qf, u16* __restrict__ Kf, u16* __restrict__ Vf,
                                                      const u16* __restrict__ wphi, u16* __restrict__ PhiQ, float* __restrict__ Part,
                                                      const u16* __restrict__ wo, const u16* __restrict__ Bwo, u16* __restrict__ WfA, u16* __restrict__ WfB, unsigned* __restrict__ flags, unsigned long long* dbg) {
  extern __shared__ __attribute__((aligned(16))) char smem[];
  const LAS char* lds = (const LAS char*)smem;
  const int tid = threadIdx.x, lane = tid & 63, wid = (int)rfl(tid >> 6), wr = wid >> 2, wc = wid & 3;
  const unsigned ldsw = (unsigned)(uintptr_t)lds + (unsigned)wid * 4096u;
  const int x = blockIdx.x & 7, j = blockIdx.x >> 3;
  auto kv_unit = [&](int k) { Unit u; u.kind = (k & 2) ? U_V : U_K; u.pn = 2 * x + (k & 1); u.pm = k >> 2; return u; };
  Unit u0, u1; bool two = true;
  if (j < 16) { u0.kind = U_Q; u0.pn = 2 * x + (j & 1); u0.pm = j >> 1; u1 = kv_unit(j); }
  else if (j < 24) { u0 = kv_unit(16 + 2 * (j - 16)); u1 = kv_unit(17 + 2 * (j - 16)); }
  else { u0.kind = U_B; u0.pn = x; u0.pm = j - 24; u1 = u0; two = false; }
  const Ptrs P = {Ax, ckv, Bx, wq, wuk, wuv, wqkv};
  Src src;
  setup_src(src, u0, P, wid);
  dma_piece<0>(src, ldsw, 0); dma_piece<4>(src, ldsw, 0); dma_piece<1>(src, ldsw, 0); dma_piece<5>(src, ldsw, 0); dma_piece<2>(src, ldsw, 0); dma_piece<6>(src, ldsw, 0); dma_piece<3>(src, ldsw, 0); dma_piece<7>(src, ldsw, 0); src_next(src);
  dma_piece<0>(src, ldsw, 1); dma_piece<4>(src, ldsw, 1); dma_piece<1>(src, ldsw, 1); dma_piece<5>(src, ldsw, 1); dma_piece<2>(src, ldsw, 1); dma_piece<6>(src, ldsw, 1); dma_piece<3>(src, ldsw, 1); dma_piece<7>(src, ldsw, 1); src_next(src);
  f32x4 acc[8][4]; Adr ad;
  Unit u = u0;
  asm volatile("s_waitcnt vmcnt(8)\n\ts_barrier" ::: "memory");
  SBAR();
  for (int ui = 0; ui < 2; ++ui) {
    const bool has_next = two && ui == 0;
    const int nt = (u.kind == U_K || u.kind == U_V) ? 8 : 10;
    setup_adr(ad, lds, u.kind == U_K || u.kind == U_V, wr, wc);
    if (u.kind == U_V) run_unit<true>(acc, ad, src, u1, P, wid, has_next, nt, ldsw);
    else run_unit<false>(acc, ad, src, u1, P, wid, has_next, nt, ldsw);
    if (u.kind == U_V) epi_v(acc, u, Vf, wr, wc);
    else if (u.kind == U_B) {
      unsigned long long bts[4] = {};
      epi_b(acc, u, lds, wphi, PhiQ, Part, wid, bts);
      BSTAMP(2);
    }
    else epi_qk(acc, u, u.kind == U_Q ? Qf : Kf, u.kind == U_Q ? QSCALE : 1.f, wr, wc);
    if (!has_next) break;
    u = u1;
  }
  if (j >= 16 && j < 24) {
    asm volatile("s_waitcnt lgkmcnt(0)\n\ts_barrier" ::: "memory");
    relayout_w(wo, Bwo, WfA, WfB, lds, wid, (x * 8 + (j - 16)) * 8 + wid, 512);
    if (blockIdx.x == 128 && wid < 4) flags[(wid * 64 + fresh_lane()) * 16] = 0u;
  }
  asm volatile("s_waitcnt vmcnt(0)" ::: "memory");
}
}

#define MFMASUM 1
namespace attn {
constexpr int LDS_K = 0, LDS_V = 65536, LDS_X = 131072, LDS_BYTES = 163840;
constexpr float L_LIMIT = 1.2676506e30f;

struct Til { f32x4 s[8]; };
struct St {
  f32x4 o[16];
  bf16x8 q[8];
  f32x4 negm[2];
  bf16x8 fr[8];
  u32x4 pw[4];
  f32x2 l[2];
  f32x4 ls[2]; bf16x8 ones;
  float m[2];
};

DI void cmask(Til& t, int thr) {
  const float NEG = -__builtin_inff();
#pragma unroll
  for (int kb = 0; kb < 4; ++kb)
#pragma unroll
    for (int qb = 0; qb < 2; ++qb)
#pragma unroll
      for (int r = 0; r < 4; ++r) if (32 * (kb >> 1) + 4 * (kb & 1) + r - 16 * qb > thr) t.s[2 * kb + qb][r] = NEG;
}

DI void safe_path(const u16* __restrict__ Qf, const u16* __restrict__ Kf, const u16* __restrict__ Vf, u16* __restrict__ Cf, int head, int blk, int rg, int grp, int lane) {
  const int g = lane >> 4, c = lane & 15;
  for (int qb = 0; qb < 2; ++qb) {
    const bf16x8* qp = (const bf16x8*)Qf + ((size_t)(head * 64 + blk * 4 + rg) * 8 + qb * 4) * 64 + lane;
    bf16x8 q[4];
#pragma unroll
    for (int d = 0; d < 4; ++d) q[d] = qp[d * 64];
    f32x4 o[4] = {};
    float m = -1e30f, l = 0.f;
    const int row = blk * 128 + rg * 32 + qb * 16 + c;
    const int nsteps = blk * 4 + rg + 1;
    for (int ks = 0; ks < nsteps; ++ks) {
      const bf16x8* kp = (const bf16x8*)Kf + ((size_t)(head * 32 + (ks >> 1)) * 16 + (ks & 1) * 8) * 64 + lane;
      f32x4 s[2] = {};
      float mx = -__builtin_inff();
#pragma unroll
      for (int b = 0; b < 2; ++b) {
#pragma unroll
        for (int d = 0; d < 4; ++d) s[b] = MFMA16(kp[(b * 4 + d) * 64], q[d], s[b]);
#pragma unroll
        for (int r = 0; r < 4; ++r) { if (ks * 32 + 8 * g + 4 * b + r > row) s[b][r] = -__builtin_inff(); mx = fmaxf(mx, s[b][r]); }
      }
      mx = xrow_max(mx);
      const float mn = fmaxf(m, mx), al = __builtin_amdgcn_exp2f(m - mn); m = mn;
      float ps = 0.f;
#pragma unroll
      for (int b = 0; b < 2; ++b)
#pragma unroll
        for (int r = 0; r < 4; ++r) { s[b][r] = __builtin_amdgcn_exp2f(s[b][r] - mn); ps += s[b][r]; }
      l = l * al + ps;
      const u32x4 w = {cvtpk(s[0][0], s[0][1]), cvtpk(s[0][2], s[0][3]), cvtpk(s[1][0], s[1][1]), cvtpk(s[1][2], s[1][3])};
      const bf16x8* vp = (const bf16x8*)Vf + ((size_t)(head * 32 + (ks >> 1)) * 16 + (ks & 1) * 8 + grp * 4) * 64 + lane;
#pragma unroll
      for (int db = 0; db < 4; ++db) { o[db] *= al; o[db] = MFMA16(vp[db * 64], __builtin_bit_cast(bf16x8, w), o[db]); }
    }
    const float il = 1.f / xrow_sum(l);
    u32x4* cp = (u32x4*)Cf + ((size_t)(head * 4 + grp * 2) * 128 + blk * 8 + rg * 2 + qb) * 64 + (c + 16 * (((g & 1) << 1) | (g >> 1)));
#pragma unroll
    for (int t = 0; t < 2; ++t)
      cp[t * 128 * 64] = pair_swap(cvtpk(o[2 * t][0] * il, o[2 * t][1] * il), cvtpk(o[2 * t][2] * il, o[2 * t][3] * il), cvtpk(o[2 * t + 1][0] * il, o[2 * t + 1][1] * il), cvtpk(o[2 * t + 1][2] * il, o[2 * t + 1][3] * il));
  }
}

#define A_RDK(n) (st.fr[(n) & 7] = lds_rd128(kp + (n) * 1024))
#define A_RDV(n) (st.fr[(n) & 7] = lds_rd128(vp + (n) * 1024))
#define RDK2(a, b) do { A_RDK(a); SBAR(); A_RDK(b); } while (0)
#define RDV2(a, b) do { A_RDV(a); SBAR(); A_RDV(b); } while (0)
#define EXP2(x) __builtin_amdgcn_exp2f(x)

#define SUMS(qb, a, b) do { } while (0)
struct Dma {
  unsigned voff; rsrc_t srdK, srdV; unsigned kdst, vdst, ksoff, vsoff; };
#define DMA_PIECE(d, j) do { if ((j) == 0) set_m0((d).kdst); if ((j) == 4) set_m0((d).vdst); \
    if ((j) < 4) dma16m<((j) & 3) * 1024>((d).voff, (d).srdK, (d).ksoff); else dma16m<((j) & 3) * 1024>((d).voff, (d).srdV, (d).vsoff); } while (0)
#define XBARL() XBARN(0)
#define XBARN(N) do { SBAR(); asm volatile("s_waitcnt vmcnt(" #N ") lgkmcnt(0)\n\ts_barrier" ::: "memory"); SBAR(); } while (0)
#define XBAR() XBARN(0)

struct Ctx {
  const u16 *Qf, *Kf, *Vf; u16* Cf;
  const LAS char *lds, *kbase, *vbase;
  unsigned lds0, wpiece;
  int lane, wid, grp, rg, head, T1, g;
};
DI void load_q(St& st, const Ctx& c, int blk) {
  const bf16x8* qp = (const bf16x8*)c.Qf + ((size_t)(c.head * 64 + blk * 4 + c.rg) * 8) * 64 + c.lane;
#pragma unroll
  for (int i = 0; i < 8; ++i) st.q[i] = qp[i * 64];
}
#define PIN_Q() asm volatile("" : "+v"(st.q[0]), "+v"(st.q[1]), "+v"(st.q[2]), "+v"(st.q[3]), "+v"(st.q[4]), "+v"(st.q[5]), "+v"(st.q[6]), "+v"(st.q[7]))
DI void dma_advance(Dma& d, const Ctx& c, int gg) {
  d.kdst ^= 32768u; d.vdst ^= 32768u; d.vsoff = d.ksoff;
  d.ksoff = (gg + 2 == c.T1) ? c.wpiece : d.ksoff + 32768u;
}

template <bool FIRST>
DI void phaseA(St& st, Til& C, const Til& P, const LAS char* kp, const LAS char* vp, const Dma& d) {
#define GA(i) do { \
    if (((i) & 3) == 0) { if (FIRST) mfma_zero(C.s[2 * ((i) >> 2)], st.fr[(i) & 7], st.q[0]); else mfma_init(C.s[2 * ((i) >> 2)], st.fr[(i) & 7], st.q[0], st.negm[0]); } \
    else mfma_acc(C.s[2 * ((i) >> 2)], st.fr[(i) & 7], st.q[(i) & 3]); \
    SBAR(); \
    if (!FIRST) {   \
      const float a_ = P.s[2 * (2 * ((i) >> 3) + (((i) & 3) >> 1)) + (((i) >> 2) & 1)][2 * ((i) & 1)], b_ = P.s[2 * (2 * ((i) >> 3) + (((i) & 3) >> 1)) + (((i) >> 2) & 1)][2 * ((i) & 1) + 1]; \
      { unsigned w_ = cvtpk(a_, b_); PIN(w_); st.pw[(i) >> 2][(i) & 3] = w_; } \
      SUMS(((i) >> 2) & 1, a_, b_); SBAR(); } \
    if (((i) & 3) == 0) { if (FIRST) mfma_zero(C.s[2 * ((i) >> 2) + 1], st.fr[(i) & 7], st.q[4]); else mfma_init(C.s[2 * ((i) >> 2) + 1], st.fr[(i) & 7], st.q[4], st.negm[1]); } \
    else mfma_acc(C.s[2 * ((i) >> 2) + 1], st.fr[(i) & 7], st.q[4 + ((i) & 3)]); \
    SBAR(); \
    if (((i) & 1) == 1) { if ((i) < 8) { RDK2(((i) + 8) & 15, ((i) + 7) & 15); } else if (!FIRST) { RDV2(((i) + 8) & 15, ((i) + 7) & 15); } } \
    if (!FIRST) { if ((i) == 0) DMA_PIECE(d, 3); if ((i) == 2) DMA_PIECE(d, 4); if ((i) == 4) DMA_PIECE(d, 5); if ((i) == 8) DMA_PIECE(d, 6); if ((i) == 10) DMA_PIECE(d, 7); } \
    SBAR(); } while (0)
  GA(0); GA(1); GA(2); GA(3); GA(4); GA(5); GA(6); GA(7); GA(8); GA(9); GA(10); GA(11); GA(12); GA(13); GA(14); GA(15);
#undef GA
}

#define MSUM(i) do { if (((i) & 7) == 2) mfma_acc(st.ls[0], st.ones, __builtin_bit_cast(bf16x8, st.pw[2 * ((i) >> 3)])); if (((i) & 7) == 5) mfma_acc(st.ls[1], st.ones, __builtin_bit_cast(bf16x8, st.pw[2 * ((i) >> 3) + 1])); } while (0)
template <class CTX> DI void phaseB(St& st, Til& C, const LAS char* vp, const LAS char* kn, Dma& d, const CTX& c, int g) {
#define GB(i) do { \
    if ((i) == 11) { XBARL(); dma_advance(d, c, g); } \
    mfma_acc(st.o[2 * ((i) & 7)], st.fr[(i) & 7], __builtin_bit_cast(bf16x8, st.pw[2 * ((i) >> 3)])); \
    SBAR(); \
    { float e_ = EXP2(C.s[(i) >> 1][2 * ((i) & 1)]); PIN(e_); C.s[(i) >> 1][2 * ((i) & 1)] = e_; } \
    SBAR(); \
    mfma_acc(st.o[2 * ((i) & 7) + 1], st.fr[(i) & 7], __builtin_bit_cast(bf16x8, st.pw[2 * ((i) >> 3) + 1])); \
    SBAR(); \
    { float e_ = EXP2(C.s[(i) >> 1][2 * ((i) & 1) + 1]); PIN(e_); C.s[(i) >> 1][2 * ((i) & 1) + 1] = e_; } \
    SBAR(); \
    MSUM(i); \
    if (((i) & 1) == 1 && (i) < 8) { RDV2(((i) + 8) & 15, ((i) + 7) & 15); } \
    if ((i) == 11) { { const LAS char* kp = kn; RDK2(1, 0); SBAR(); RDK2(3, 2); } } \
    if ((i) == 12) { DMA_PIECE(d, 0); } \
    if ((i) == 13) { { const LAS char* kp = kn; RDK2(5, 4); } } \
    if ((i) == 14) { DMA_PIECE(d, 1); DMA_PIECE(d, 2); } \
    if ((i) == 15) { { const LAS char* kp = kn; RDK2(7, 6); } } \
    SBAR(); } while (0)
  GB(0); GB(1); GB(2); GB(3); GB(4); GB(5); GB(6); GB(7); GB(8); GB(9); GB(10); GB(11); GB(12); GB(13); GB(14); GB(15);
#undef GB
}

template <int BI>
DI void do_block(St& st, Dma& d, Ctx& c, const int blk, const int next_blk) {
  const int T = blk + 1, lane = c.lane, grp = c.grp, rg = c.rg, wid = c.wid;
  int g = c.g;
  const int thr = rg * 32 + (lane & 15) - grp * 64 - 8 * (lane >> 4);
  Til a, b;
  { const LAS char* kp = c.kbase + (g & 1) * 32768;
    A_RDK(1); A_RDK(0); A_RDK(3); A_RDK(2); A_RDK(5); A_RDK(4); A_RDK(7); A_RDK(6);
    phaseA<true>(st, a, a, kp, kp, d);
    MFMA_SETTLE();
    if (T == 1) cmask(a, thr);
#pragma unroll
    for (int qb = 0; qb < 2; ++qb) {
      float mx = max3f(a.s[qb][0], a.s[qb][1], a.s[qb][2]), my = max3f(a.s[qb][3], a.s[2 + qb][0], a.s[2 + qb][1]);
      mx = max3f(mx, a.s[2 + qb][2], a.s[2 + qb][3]); my = max3f(my, a.s[4 + qb][0], a.s[4 + qb][1]);
      mx = max3f(mx, a.s[4 + qb][2], a.s[4 + qb][3]); my = max3f(my, a.s[6 + qb][0], a.s[6 + qb][1]);
      mx = max3f(mx, a.s[6 + qb][2], a.s[6 + qb][3]); mx = max3f(mx, my, -1e30f);
      st.m[qb] = xrow_max(mx); st.l[qb] = (f32x2){0.f, 0.f}; st.ls[qb] = (f32x4){};
    }
    XBAR();
    dma_advance(d, c, g);
    DMA_PIECE(d, 0); DMA_PIECE(d, 1); DMA_PIECE(d, 2);
    { const LAS char* kp = c.kbase + ((g + 1) & 1) * 32768; A_RDK(1); A_RDK(0); A_RDK(3); A_RDK(2); A_RDK(5); A_RDK(4); A_RDK(7); A_RDK(6); }
#pragma unroll
    for (int qb = 0; qb < 2; ++qb) { st.negm[qb] = (f32x4){-st.m[qb], -st.m[qb], -st.m[qb], -st.m[qb]};
#pragma unroll
      for (int kb = 0; kb < 4; ++kb)
#pragma unroll
        for (int r = 0; r < 4; ++r) a.s[2 * kb + qb][r] = EXP2(a.s[2 * kb + qb][r] - st.m[qb]); }
#pragma unroll
    for (int i = 0; i < 16; ++i) st.o[i] = (f32x4){};
    asm volatile("" : "+v"(st.o[0]), "+v"(st.o[1]), "+v"(st.o[2]), "+v"(st.o[3]), "+v"(st.o[4]), "+v"(st.o[5]), "+v"(st.o[6]), "+v"(st.o[7]));
    asm volatile("" : "+v"(st.o[8]), "+v"(st.o[9]), "+v"(st.o[10]), "+v"(st.o[11]), "+v"(st.o[12]), "+v"(st.o[13]), "+v"(st.o[14]), "+v"(st.o[15]));
    asm volatile("s_nop 1" : "+v"(st.negm[0]), "+v"(st.negm[1]), "+v"(st.ls[0]), "+v"(st.ls[1]));
    ++g;
  }
#define STEP(C, P, t, kp_, vp_, kn_) do { \
    phaseA<false>(st, C, P, kp_, vp_, d); \
    if ((t) == T - 1) { MFMA_SETTLE(); cmask(C, thr); } \
    SBAR(); \
    phaseB(st, C, vp_, kn_, d, c, g); \
    ++g; } while (0)
  int t = 1;
  const LAS char* kE = c.kbase + (g & 1) * 32768; const LAS char* kO = c.kbase + ((g + 1) & 1) * 32768;
  const LAS char* vE = c.vbase + ((g + 1) & 1) * 32768; const LAS char* vO = c.vbase + (g & 1) * 32768;
  for (; t + 1 < T; t += 2) { STEP(b, a, t, kE, vE, kO); STEP(a, b, t + 1, kO, vO, kE); }
  if (t < T) { STEP(b, a, t, kE, vE, kO); a = b; }
#undef STEP
  DMA_PIECE(d, 3); DMA_PIECE(d, 4); DMA_PIECE(d, 5); DMA_PIECE(d, 6); DMA_PIECE(d, 7);
  if (BI == 0) load_q(st, c, next_blk);
  { const LAS char* vp = c.vbase + ((g - 1) & 1) * 32768;
    A_RDV(1); A_RDV(0); A_RDV(3); A_RDV(2); A_RDV(5); A_RDV(4); A_RDV(7); A_RDV(6);
#pragma unroll
    for (int i = 0; i < 16; ++i) { const float x = a.s[2 * (2 * (i >> 3) + ((i & 3) >> 1)) + ((i >> 2) & 1)][2 * (i & 1)], y = a.s[2 * (2 * (i >> 3) + ((i & 3) >> 1)) + ((i >> 2) & 1)][2 * (i & 1) + 1];
      st.pw[i >> 2][i & 3] = cvtpk(x, y);
    }
#define GD(i) do { mfma_acc(st.o[2 * ((i) & 7)], st.fr[(i) & 7], __builtin_bit_cast(bf16x8, st.pw[2 * ((i) >> 3)])); \
      mfma_acc(st.o[2 * ((i) & 7) + 1], st.fr[(i) & 7], __builtin_bit_cast(bf16x8, st.pw[2 * ((i) >> 3) + 1])); MSUM(i); \
      if (((i) & 1) == 1 && (i) < 8) { RDV2(((i) + 8) & 15, ((i) + 7) & 15); } SBAR(); } while (0)
    asm volatile("s_nop 1" : "+v"(st.pw[0]), "+v"(st.pw[1]), "+v"(st.pw[2]), "+v"(st.pw[3]));
    GD(0); GD(1); GD(2); GD(3); GD(4); GD(5); GD(6); GD(7); GD(8); GD(9); GD(10); GD(11); GD(12); GD(13); GD(14); GD(15);
    MFMA_SETTLE();
#undef GD
  }
  float l[2] = {st.ls[0][0], st.ls[1][0]};
  LAS char* xr = (LAS char*)c.lds + LDS_X;
  *(LAS f32x4*)(xr + wid * 1024 + lane * 16) = (f32x4){st.m[0], l[0], st.m[1], l[1]};
  asm volatile("s_waitcnt lgkmcnt(0)\n\ts_barrier" ::: "memory");
  const f32x4 oth = *(const LAS f32x4*)(xr + (wid ^ 4) * 1024 + lane * 16);
  bool bad = false; float fs[2];
#pragma unroll
  for (int qb = 0; qb < 2; ++qb) {
    const float M = fmaxf(st.m[qb], oth[2 * qb]);
    const float fo = EXP2(st.m[qb] - M), fp = EXP2(oth[2 * qb] - M);
    const float lt = l[qb] * fo + oth[2 * qb + 1] * fp;
    bad = bad || !(l[qb] < L_LIMIT) || !(oth[2 * qb + 1] < L_LIMIT);
    fs[qb] = fo * __builtin_amdgcn_rcpf(lt);
  }
  LAS char* vslot = (LAS char*)c.lds + LDS_V + ((g - 1) & 1) * 32768;
  LAS char* sendp = vslot + wid * 4096 + lane * 16;
  const LAS char* recvp = vslot + (wid ^ 4) * 4096 + lane * 16;
  f32x4 k[8];
#define PACK8(x, y, f) (u32x4){cvtpk((x)[0] * (f), (x)[1] * (f)), cvtpk((x)[2] * (f), (x)[3] * (f)), cvtpk((y)[0] * (f), (y)[1] * (f)), cvtpk((y)[2] * (f), (y)[3] * (f))}
  if (grp == 0) {
#pragma unroll
    for (int i = 0; i < 4; ++i) *(LAS u32x4*)(sendp + i * 1024) = PACK8(st.o[8 + 4 * (i >> 1) + (i & 1)], st.o[8 + 4 * (i >> 1) + 2 + (i & 1)], fs[i & 1]);
#pragma unroll
    for (int i = 0; i < 8; ++i) k[i] = st.o[i];
  } else {
#pragma unroll
    for (int i = 0; i < 4; ++i) *(LAS u32x4*)(sendp + i * 1024) = PACK8(st.o[4 * (i >> 1) + (i & 1)], st.o[4 * (i >> 1) + 2 + (i & 1)], fs[i & 1]);
#pragma unroll
    for (int i = 0; i < 8; ++i) k[i] = st.o[8 + i];
  }
  asm volatile("s_waitcnt lgkmcnt(0)\n\ts_barrier" ::: "memory");
  float amax = 0.f;
  u32x4 res[4];
#pragma unroll
  for (int i = 0; i < 4; ++i) {
    const u32x4 x = *(const LAS u32x4*)(recvp + i * 1024);
    const f32x4 p = k[4 * (i >> 1) + (i & 1)], q = k[4 * (i >> 1) + 2 + (i & 1)]; const float f = fs[i & 1];
    const float v0 = p[0] * f + bf_lo(x[0]), v1 = p[1] * f + bf_hi(x[0]), v2 = p[2] * f + bf_lo(x[1]), v3 = p[3] * f + bf_hi(x[1]);
    const float v4 = q[0] * f + bf_lo(x[2]), v5 = q[1] * f + bf_hi(x[2]), v6 = q[2] * f + bf_lo(x[3]), v7 = q[3] * f + bf_hi(x[3]);
    amax = max3f(amax, __builtin_fabsf(v0), __builtin_fabsf(v1)); amax = max3f(amax, __builtin_fabsf(v2), __builtin_fabsf(v3));
    amax = max3f(amax, __builtin_fabsf(v4), __builtin_fabsf(v5)); amax = max3f(amax, __builtin_fabsf(v6), __builtin_fabsf(v7));
    res[i] = pair_swap(cvtpk(v0, v1), cvtpk(v2, v3), cvtpk(v4, v5), cvtpk(v6, v7));
  }
  bad = bad || !(amax < 3.0e38f);
  if (__builtin_expect(__any(bad), 0)) {
    safe_path(c.Qf, c.Kf, c.Vf, c.Cf, c.head, blk, rg, grp, lane);
  } else {
#pragma unroll
    for (int i = 0; i < 4; ++i) ((u32x4*)c.Cf)[((size_t)(c.head * 4 + grp * 2 + (i >> 1)) * 128 + blk * 8 + rg * 2 + (i & 1)) * 64 + ((lane & 15) + 16 * ((((lane >> 4) & 1) << 1) | (lane >> 5)))] = res[i];
  }
  c.g = g;
}

struct BIn { f32x4 part[8]; bf16x8 q0[2], q1[2]; };
DI void b2_load(BIn& b, const u16* __restrict__ PhiQ, const float* __restrict__ Part, int head, int pp, int wid, int lane) {
  const int c = lane & 15, g = lane >> 4;
  if (wid < 6) {
#pragma unroll
    for (int pm = 0; pm < 8; ++pm) b.part[pm] = ((const f32x4*)Part)[(((size_t)pm * 32 + head) * 6 + wid) * 64 + lane];
  }
#pragma unroll
  for (int i = 0; i < 2; ++i) { const char* src = (const char*)PhiQ + ((size_t)head * 128 + pp * 16 + wid * 2 + i) * 1280;
    b.q0[i] = *(const bf16x8*)(src + lane * 16);
    b.q1[i] = *(const bf16x8*)(src + 1024 + c * 16);
    if (g != 0) b.q1[i] = (bf16x8){0, 0, 0, 0, 0, 0, 0, 0}; }
}
DI void b2_publish(const BIn& b, LAS char* xr, int wid, int lane) {
  if (wid < 6) { const f32x4 s = ((b.part[0] + b.part[1]) + (b.part[2] + b.part[3])) + ((b.part[4] + b.part[5]) + (b.part[6] + b.part[7]));
    *(LAS u32x2*)(xr + wid * 512 + lane * 8) = (u32x2){cvtpk(s[0], s[1]), cvtpk(s[2], s[3])}; }
}
DI void b2_finish(const BIn& b, const LAS char* xr, u16* __restrict__ Of, int head, int pp, int wid, int lane) {
  const int c = lane & 15, g = lane >> 4;
  bf16x8 a0[2], a1[2];
#pragma unroll
  for (int db = 0; db < 2; ++db) {
    a0[db] = __builtin_shufflevector(*(const LAS s16x4*)(xr + db * 512 + lane * 8), *(const LAS s16x4*)(xr + (2 + db) * 512 + lane * 8), 0, 1, 2, 3, 4, 5, 6, 7);
    a1[db] = __builtin_shufflevector(*(const LAS s16x4*)(xr + (4 + db) * 512 + c * 8), *(const LAS s16x4*)(xr + (4 + db) * 512 + (c + 16) * 8), 0, 1, 2, 3, 4, 5, 6, 7);
    if (g != 0) a1[db] = (bf16x8){0, 0, 0, 0, 0, 0, 0, 0};
  }
#pragma unroll
  for (int i = 0; i < 2; ++i) {
    f32x4 n0 = MFMA16(a0[0], b.q0[i], ((f32x4){})); n0 = MFMA16(a1[0], b.q1[i], n0);
    f32x4 n1 = MFMA16(a0[1], b.q0[i], ((f32x4){})); n1 = MFMA16(a1[1], b.q1[i], n1);
    const float inv = 1.f / (__shfl(n1[0], c + 16) + 1e-6f);
    char* base = (char*)Of + (size_t)(pp * 16 + wid * 2 + i) * 1024;
    { const int k = head * 20 + 4 * g; *(u32x2*)(base + (size_t)(k >> 5) * 131072 + (c + 16 * ((k >> 3) & 3)) * 16 + 8 * ((k >> 2) & 1)) = (u32x2){cvtpk(n0[0] * inv, n0[1] * inv), cvtpk(n0[2] * inv, n0[3] * inv)}; }
    if (g == 0) { const int k = head * 20 + 16; *(u32x2*)(base + (size_t)(k >> 5) * 131072 + (c + 16 * ((k >> 3) & 3)) * 16 + 8 * ((k >> 2) & 1)) = (u32x2){cvtpk(n1[0] * inv, n1[1] * inv), cvtpk(n1[2] * inv, n1[3] * inv)}; }
  }
}

__global__ __launch_bounds__(512, 2) void attn_kernel(const u16* __restrict__ Qf, const u16* __restrict__ Kf, const u16* __restrict__ Vf, u16* __restrict__ Cf,
                                                      const u16* __restrict__ PhiQ, const float* __restrict__ Part, u16* __restrict__ Of, unsigned long long* dbg) {
  extern __shared__ __attribute__((aligned(16))) char smem[];
  Ctx c; c.Qf = Qf; c.Kf = Kf; c.Vf = Vf; c.Cf = Cf;
  c.lds = (const LAS char*)smem; c.lds0 = (unsigned)(uintptr_t)c.lds;
  const int tid = threadIdx.x; c.lane = tid & 63;
  c.wid = (int)rfl(tid >> 6); c.grp = c.wid >> 2; c.rg = c.wid & 3;
  const int xcd = blockIdx.x & 7, jj = blockIdx.x >> 3, pp = jj & 7; c.head = xcd * 4 + (jj >> 3);
  c.T1 = 16 - pp; c.g = 0;
  c.wpiece = (unsigned)c.wid * 4096u;
  c.kbase = c.lds + LDS_K + c.grp * 16384 + c.lane * 16; c.vbase = c.lds + LDS_V + c.grp * 16384 + c.lane * 16;
  Dma d;
  d.voff = (unsigned)c.lane * 16u;
  d.srdK = mk_rsrc(Kf + (size_t)c.head * 262144, 524288u); d.srdV = mk_rsrc(Vf + (size_t)c.head * 262144, 524288u);
  St st;
  st.ones = (bf16x8){0x3F80, 0x3F80, 0x3F80, 0x3F80, 0x3F80, 0x3F80, 0x3F80, 0x3F80}; PIN(st.ones);
  d.kdst = c.lds0 + LDS_K + c.wpiece; d.ksoff = c.wpiece; d.vdst = c.lds0 + LDS_V + c.wpiece; d.vsoff = c.wpiece;
  DMA_PIECE(d, 0); DMA_PIECE(d, 1); DMA_PIECE(d, 2); DMA_PIECE(d, 3);
  load_q(st, c, 15 - pp);
  { BIn bi; b2_load(bi, PhiQ, Part, c.head, pp, c.wid, c.lane);
    b2_publish(bi, (LAS char*)c.lds + LDS_X, c.wid, c.lane);
    PIN_Q();
    XBAR();
    DMA_PIECE(d, 4); DMA_PIECE(d, 5); DMA_PIECE(d, 6); DMA_PIECE(d, 7);
    d.kdst = c.lds0 + LDS_K + 32768 + c.wpiece; d.ksoff = 32768u + c.wpiece;
    DMA_PIECE(d, 0); DMA_PIECE(d, 1); DMA_PIECE(d, 2); DMA_PIECE(d, 3);
    b2_finish(bi, c.lds + LDS_X, Of, c.head, pp, c.wid, c.lane); }
  do_block<0>(st, d, c, 15 - pp, pp);
  PIN_Q();
  do_block<1>(st, d, c, pp, pp);
  asm volatile("s_waitcnt vmcnt(0)" ::: "memory");
}
}

namespace outp {
constexpr int SLOTB = 40960, LDS_BYTES = 4 * SLOTB;
struct Src { rsrc_t X, W; unsigned sX, sW; };
struct XSet { bf16x8 f[2][2]; };
struct WSet { bf16x8 f[10]; };
DI void ldx(bf16x8& d, unsigned voff, rsrc_t r, unsigned soff) { asm volatile("buffer_load_dwordx4 %0, %1, %2, %3 offen" : "=v"(d) : "v"(voff), "s"(r), "s"(soff) : "memory"); }
DI void ldx1k(bf16x8& d, unsigned voff, rsrc_t r, unsigned soff) { asm volatile("buffer_load_dwordx4 %0, %1, %2, %3 offen offset:1024" : "=v"(d) : "v"(voff), "s"(r), "s"(soff) : "memory"); }
#define RW(set, p, ks, n) (set.f[n] = lds_rd128((p) + (ks) * 10240 + (n) * 1024))
template <int I> DI void wp(const Src& s, unsigned ldsw, unsigned voff) {
  if constexpr (I == 0 || I == 2) set_m0(ldsw);
  if constexpr (I == 4) set_m0(ldsw + 4096);
  dma16m<(I & 3) * 1024>(voff, s.W, s.sW + (I >> 2) * 4096u);
}
DI void round(f32x4 (&acc)[2][10], XSet& X, WSet& wA, WSet& wB, const LAS char* wc, const LAS char* wn_, Src& src, const Src& srcA, unsigned lw2, unsigned lw3, unsigned voff,
              bool h2, bool h3, int wait, bool swx, bool sww) {
#define MM(k, W_, m, n) do { mfma_acc(acc[m][n], W_.f[n], X.f[k][m]); SBAR(); } while (0)
#define HX(k, m) do { if (h2) { if (m) ldx1k(X.f[k][1], voff, src.X, src.sX + (k) * 262144u); else ldx(X.f[k][0], voff, src.X, src.sX + (k) * 262144u); } SBAR(); } while (0)
#define HW2(i) do { if (h2) wp<i>(src, lw2, voff); SBAR(); } while (0)
#define HW3(i) do { if (h3) wp<i>(src, lw3, voff); SBAR(); } while (0)
#define NOP_() do { } while (0)
#define KSTEP(k, CUR, NXT, p, A, B, C, D) do { \
    MM(k, CUR, 0, 0); RW(NXT, p, 0, 0); SBAR(); MM(k, CUR, 1, 0); MM(k, CUR, 0, 1); RW(NXT, p, 0, 1); SBAR(); MM(k, CUR, 1, 1); A; MM(k, CUR, 0, 2); RW(NXT, p, 0, 2); SBAR(); MM(k, CUR, 1, 2); \
    MM(k, CUR, 0, 3); RW(NXT, p, 0, 3); SBAR(); MM(k, CUR, 1, 3); B; MM(k, CUR, 0, 4); RW(NXT, p, 0, 4); SBAR(); MM(k, CUR, 1, 4); MM(k, CUR, 0, 5); RW(NXT, p, 0, 5); SBAR(); MM(k, CUR, 1, 5); C; \
    MM(k, CUR, 0, 6); RW(NXT, p, 0, 6); SBAR(); MM(k, CUR, 1, 6); MM(k, CUR, 0, 7); RW(NXT, p, 0, 7); SBAR(); MM(k, CUR, 1, 7); D; MM(k, CUR, 0, 8); RW(NXT, p, 0, 8); SBAR(); MM(k, CUR, 1, 8); \
    MM(k, CUR, 0, 9); RW(NXT, p, 0, 9); SBAR(); MM(k, CUR, 1, 9); } while (0)
  if (swx) src.sX = srcA.sX, src.X = srcA.X;
  KSTEP(0, wA, wB, wc + 20480, HW2(2), HW2(3), HW2(4), NOP_());
  HX(0, 0);
  if (h2) src.sW += 4u * 40960u;
  if (wait == 8) asm volatile("s_waitcnt vmcnt(8) lgkmcnt(0)\n\ts_barrier" ::: "memory");
  else if (wait == 6) asm volatile("s_waitcnt vmcnt(6) lgkmcnt(0)\n\ts_barrier" ::: "memory");
  else asm volatile("s_waitcnt vmcnt(0) lgkmcnt(0)\n\ts_barrier" ::: "memory");
  SBAR();
  if (sww) src.sW = srcA.sW, src.W = srcA.W;
  KSTEP(1, wB, wA, wn_, HX(0, 1), HW3(0), HW3(1), NOP_());
  HX(1, 0); HX(1, 1);
  if (h2) src.sX += 4u * 131072u;
#undef NOP_
#undef HW3
#undef HW2
#undef HX
#undef KSTEP
#undef MM
}
#define PIN_X(XS) asm volatile("" : "+v"(XS.f[0][0]), "+v"(XS.f[0][1]), "+v"(XS.f[1][0]), "+v"(XS.f[1][1]))
#define PIN_ACC() do { asm volatile("" : "+v"(acc[0][0]), "+v"(acc[0][1]), "+v"(acc[0][2]), "+v"(acc[0][3]), "+v"(acc[0][4]), "+v"(acc[0][5]), "+v"(acc[0][6]), "+v"(acc[0][7]), "+v"(acc[0][8]), "+v"(acc[0][9])); \
    asm volatile("s_nop 1" : "+v"(acc[1][0]), "+v"(acc[1][1]), "+v"(acc[1][2]), "+v"(acc[1][3]), "+v"(acc[1][4]), "+v"(acc[1][5]), "+v"(acc[1][6]), "+v"(acc[1][7]), "+v"(acc[1][8]), "+v"(acc[1][9])); SBAR(); } while (0)
#define RLX_AGENT __ATOMIC_RELAXED, __HIP_MEMORY_SCOPE_AGENT
DI u32x2 pack4(const f32x4& a) { return (u32x2){cvtpk(a[0], a[1]), cvtpk(a[2], a[3])}; }
DI f32x4 unpack4(unsigned x, unsigned y) { return (f32x4){bf_lo(x), bf_hi(x), bf_lo(y), bf_hi(y)}; }

__global__ __launch_bounds__(512, 2) void outp_kernel(const u16* __restrict__ Cf, const u16* __restrict__ Of, const u16* __restrict__ WfA, const u16* __restrict__ WfB,
                                                      u16* __restrict__ out, u16* __restrict__ Ex, unsigned* __restrict__ flags, unsigned long long* dbg) {
#define OST(i)
  extern __shared__ __attribute__((aligned(16))) char smem[];
  const LAS char* lds = (const LAS char*)smem;
  const int tid = threadIdx.x, lane = tid & 63, wid = (int)rfl(tid >> 6), par = wid >> 2, wm = wid & 3;
  const int x = blockIdx.x & 7, j = blockIdx.x >> 3, kq = x >> 1, pm = 8 * (x & 1) + (j >> 2), pn = j & 3, tile = pm * 4 + pn;
  const unsigned ldsw = (unsigned)(uintptr_t)lds + (unsigned)wid * 5120u, voff = (unsigned)lane * 16u;
  const int a0 = kq == 0 ? 0 : (kq == 1 ? 3 : (kq == 2 ? 13 : 23)), R = kq == 0 ? 8 : (kq == 3 ? 9 : 10);
  const unsigned ox = (unsigned)(par * 128 + pm * 8 + wm * 2) * 1024u, ow = (unsigned)((wid >> 1) * 40 + pn * 10 + (wid & 1) * 5) * 1024u;
  Src srcA, src;
  srcA.X = mk_rsrc(Cf, 2048u * 4096u * 2u); srcA.W = mk_rsrc(WfA, 4096u * 640u * 2u);
  srcA.sX = (unsigned)(a0 * 4 * 128) * 1024u + ox; srcA.sW = (unsigned)(a0 * 4 * 40) * 1024u + ow;
  src.X = mk_rsrc(kq == 0 ? Of : Cf, kq == 0 ? 2048u * 640u * 2u : 2048u * 4096u * 2u); src.W = mk_rsrc(kq == 0 ? WfB : WfA, kq == 0 ? 640u * 640u * 2u : 4096u * 640u * 2u);
  src.sX = kq == 0 ? ox : srcA.sX; src.sW = kq == 0 ? ow : srcA.sW;
  XSet X0, X1;
#define FETCH(XS, slot) do { _Pragma("unroll") for (int k = 0; k < 2; ++k) { ldx(XS.f[k][0], voff, src.X, src.sX + k * 262144u); ldx1k(XS.f[k][1], voff, src.X, src.sX + k * 262144u); } \
    wp<0>(src, ldsw + (slot) * SLOTB, voff); wp<1>(src, ldsw + (slot) * SLOTB, voff); wp<2>(src, ldsw + (slot) * SLOTB, voff); wp<3>(src, ldsw + (slot) * SLOTB, voff); wp<4>(src, ldsw + (slot) * SLOTB, voff); \
    src.sX += 4u * 131072u; src.sW += 4u * 40960u; } while (0)
  FETCH(X0, 0); FETCH(X1, 1);
#undef FETCH
  wp<0>(src, ldsw + 2 * SLOTB, voff); wp<1>(src, ldsw + 2 * SLOTB, voff);
  const LAS char* wa = lds + par * 10240 + lane * 16;
  f32x4 acc[2][10];
#pragma unroll
  for (int m = 0; m < 2; ++m)
#pragma unroll
    for (int n = 0; n < 10; ++n) acc[m][n] = (f32x4){};
  PIN_ACC();
  WSet wA, wB;
  asm volatile("s_waitcnt vmcnt(11)\n\ts_barrier" ::: "memory"); SBAR();
  OST(0);
#pragma unroll
  for (int n = 0; n < 10; ++n) RW(wA, wa, 0, n);
  const int c = lane & 15, g = lane >> 4;
  const rsrc_t rout = mk_rsrc(out, 2048u * 1280u * 2u);
  const unsigned vout = (unsigned)((pm * 128 + wm * 32 + par * 16 + c) * 2560 + (pn * 160 + 4 * g) * 2);
  u32x2 bres[10];
#pragma unroll
  for (int t = 0; t < 10; ++t) bres[t] = (u32x2){0u, 0u};
  unsigned o0 = 0, o1 = SLOTB, o2 = 2 * SLOTB;
  for (int s = 0; s < R; s += 2) {
    PIN_X(X0);
    round(acc, X0, wA, wB, wa + o0, wa + o1, src, srcA, ldsw + o2, ldsw + o0, voff, s + 2 < R, s + 3 < R, s == 0 ? 6 : (s + 2 < R ? 8 : 0), false, kq == 0 && s == 2);
    if (kq == 0 && s == 4) {
      MFMA_SETTLE();
      LAS char* mw = (LAS char*)lds + 3 * SLOTB + wid * 5120 + lane * 8; const LAS char* mr = lds + 3 * SLOTB + (wid ^ 4) * 5120 + lane * 8;
#pragma unroll
      for (int n = 0; n < 10; ++n) *(LAS u32x2*)(mw + n * 512) = pack4(par == 0 ? acc[1][n] : acc[0][n]);
      asm volatile("s_waitcnt lgkmcnt(0)\n\ts_barrier" ::: "memory");
#pragma unroll
      for (int n = 0; n < 10; ++n) { const u32x2 o = *(const LAS u32x2*)(mr + n * 512); bres[n] = pack4((par == 0 ? acc[0][n] : acc[1][n]) + unpack4(o[0], o[1])); acc[0][n] = (f32x4){}; acc[1][n] = (f32x4){}; }
      PIN_ACC();
    }
    if (s + 1 < R) {
      PIN_X(X1);
      round(acc, X1, wA, wB, wa + o1, wa + o2, src, srcA, ldsw + o0, ldsw + o1, voff, s + 3 < R, s + 4 < R, s + 3 < R ? 8 : 0, kq == 0 && s == 2, false);
    }
    { const unsigned t = o0; o0 = o2; o2 = o1; o1 = t; }
  }
  MFMA_SETTLE();
  OST(1);
  f32x4 own[10];
  asm volatile("s_barrier" ::: "memory");
  { LAS char* mw = (LAS char*)lds + wid * 10240 + lane * 16; const LAS char* mr = lds + (wid ^ 4) * 10240 + lane * 16;
#pragma unroll
    for (int n = 0; n < 10; ++n) { *(LAS f32x4*)(mw + n * 1024) = par == 0 ? acc[1][n] : acc[0][n]; own[n] = par == 0 ? acc[0][n] : acc[1][n]; }
    asm volatile("s_waitcnt lgkmcnt(0)\n\ts_barrier" ::: "memory");
#pragma unroll
    for (int n = 0; n < 10; ++n) own[n] += *(const LAS f32x4*)(mr + n * 1024);
  }
  const rsrc_t rex = mk_rsrc(Ex, 64u * 4u * 8u * 4u * 2048u);
  { const unsigned sb = (unsigned)(((tile * 4 + kq) * 8 + wid) * 4) * 2048u;
#define SEND(o) do { if (kq != (o)) { const u32x2 p0 = pack4(own[o]), p1 = pack4(own[(o) + 4]); st128<16>((u32x4){p0[0], p0[1], p1[0], p1[1]}, rex, sb + (o) * 2048u + lane * 16u); \
      if ((o) < 2) st64<16>(pack4(own[((o) + 8) % 10]), rex, sb + (o) * 2048u + 1024u + lane * 8u); } } while (0)
    SEND(0); SEND(1); SEND(2); SEND(3);
#undef SEND
  }
  asm volatile("s_waitcnt vmcnt(0)\n\ts_barrier" ::: "memory");
  OST(2);
  if (tid == 0) __hip_atomic_store(flags + (tile * 4 + kq) * 16, 1u, RLX_AGENT);
  if (kq == 0) {
#pragma unroll
    for (int n = 0; n < 10; ++n) st64<0>(bres[n], rout, vout + 1280u + n * 32u);
  }
  if (wid == 0) {
    const unsigned long long t0 = __builtin_amdgcn_s_memrealtime();
    for (;;) {
      const unsigned v = lane < 4 ? __hip_atomic_load(flags + (tile * 4 + lane) * 16, RLX_AGENT) : 1u;
      if (__all(v == 1u)) break;
      if (__builtin_amdgcn_s_memrealtime() - t0 > 200000ull) break;
      __builtin_amdgcn_s_sleep(1);
    }
  }
  asm volatile("s_barrier" ::: "memory");
  OST(3);
  u32x4 rp[3]; u32x2 rs[3];
#pragma unroll
  for (int i = 0; i < 3; ++i) { const int src_ = (kq + 1 + i) & 3; const unsigned sb = (unsigned)(((tile * 4 + src_) * 8 + wid) * 4 + kq) * 2048u;
    rp[i] = __builtin_amdgcn_raw_buffer_load_b128(rex, sb + lane * 16u, 0, 16);
    rs[i] = __builtin_amdgcn_raw_buffer_load_b64(rex, kq < 2 ? sb + 1024u + lane * 8u : 0x80000000u, 0, 16); }
  f32x4 f0 = kq == 0 ? own[0] : kq == 1 ? own[1] : kq == 2 ? own[2] : own[3], f1 = kq == 0 ? own[4] : kq == 1 ? own[5] : kq == 2 ? own[6] : own[7], f2 = kq == 0 ? own[8] : own[9];
#pragma unroll
  for (int i = 0; i < 3; ++i) { f0 += unpack4(rp[i][0], rp[i][1]); f1 += unpack4(rp[i][2], rp[i][3]); f2 += unpack4(rs[i][0], rs[i][1]); }
  st64<0>(pack4(f0), rout, vout + (unsigned)kq * 32u); st64<0>(pack4(f1), rout, vout + (unsigned)(kq + 4) * 32u); st64<0>(pack4(f2), rout, kq < 2 ? vout + (unsigned)(kq + 8) * 32u : 0x80000000u);
}
}

extern "C" void kernel_launch(void* const* d_in, const int* in_sizes, int n_in, void* d_out, int out_size, void* d_ws, size_t ws_size, hipStream_t stream) {
  const u16* Ax = (const u16*)d_in[0]; const u16* ckv = (const u16*)d_in[1]; const u16* wq = (const u16*)d_in[2]; const u16* wuk = (const u16*)d_in[3];
  const u16* wuv = (const u16*)d_in[4]; const u16* wo = (const u16*)d_in[5]; const u16* Bx = (const u16*)d_in[6]; const u16* wqkv = (const u16*)d_in[7];
  const u16* wphi = (const u16*)d_in[8]; const u16* Bwo = (const u16*)d_in[9]; u16* out = (u16*)d_out;
  char* ws = (char*)d_ws;
  u16* Qf = (u16*)(ws); u16* Kf = (u16*)(ws + (16u << 20)); u16* Vf = (u16*)(ws + (32u << 20)); u16* Cf = (u16*)(ws + (48u << 20));
  u16* PhiQ = (u16*)(ws + (64u << 20)); u16* Of = (u16*)(ws + (72u << 20)); float* Part = (float*)(ws + (76u << 20));
  u16* WfA = (u16*)(ws + (80u << 20)); u16* WfB = (u16*)(ws + (86u << 20)); unsigned* flags = (unsigned*)(ws + (88u << 20)); u16* Ex = (u16*)(ws + (90u << 20));
  (void)hipFuncSetAttribute((const void*)proj::proj_kernel, hipFuncAttributeMaxDynamicSharedMemorySize, proj::LDS_BYTES);
  (void)hipFuncSetAttribute((const void*)attn::attn_kernel, hipFuncAttributeMaxDynamicSharedMemorySize, attn::LDS_BYTES);
  (void)hipFuncSetAttribute((const void*)outp::outp_kernel, hipFuncAttributeMaxDynamicSharedMemorySize, outp::LDS_BYTES);
  proj::proj_kernel<<<256, 512, proj::LDS_BYTES, stream>>>(Ax, ckv, wq, wuk, wuv, Bx, wqkv, Qf, Kf, Vf, wphi, PhiQ, Part, wo, Bwo, WfA, WfB, flags, nullptr);
  attn::attn_kernel<<<256, 512, attn::LDS_BYTES, stream>>>(Qf, Kf, Vf, Cf, PhiQ, Part, Of, nullptr);
  outp::outp_kernel<<<256, 512, outp::LDS_BYTES, stream>>>(Cf, Of, WfA, WfB, out, Ex, flags, nullptr);
}
```

```cpp
#include <hip/hip_runtime.h>
#include <stdint.h>
typedef unsigned short u16;
typedef short bf16x8 __attribute__((ext_vector_type(8)));
typedef short s16x4 __attribute__((ext_vector_type(4)));
typedef float f32x4 __attribute__((ext_vector_type(4)));
typedef float f32x2 __attribute__((ext_vector_type(2)));
typedef __bf16 bf16x2_t __attribute__((ext_vector_type(2)));
typedef unsigned u32x4 __attribute__((ext_vector_type(4)));
typedef unsigned u32x2 __attribute__((ext_vector_type(2)));
typedef __amdgpu_buffer_rsrc_t rsrc_t;
#define LAS __attribute__((address_space(3)))
#define DI __device__ __forceinline__
#define SBAR() __builtin_amdgcn_sched_barrier(0)
#define PIN(x) asm volatile("" : "+v"(x))
#define MFMA16(a, b, c) __builtin_amdgcn_mfma_f32_16x16x32_bf16(a, b, c, 0, 0, 0)
DI void mfma_acc(f32x4& acc, const bf16x8& a, const bf16x8& b) { asm volatile("v_mfma_f32_16x16x32_bf16 %0, %1, %2, %0" : "+v"(acc) : "v"(a), "v"(b)); }
DI void mfma_init(f32x4& d, const bf16x8& a, const bf16x8& b, const f32x4& c) { asm volatile("v_mfma_f32_16x16x32_bf16 %0, %1, %2, %3" : "=&v"(d) : "v"(a), "v"(b), "v"(c)); }
DI void mfma_zero(f32x4& d, const bf16x8& a, const bf16x8& b) { asm volatile("v_mfma_f32_16x16x32_bf16 %0, %1, %2, 0" : "=&v"(d) : "v"(a), "v"(b)); }
#define MFMA_SETTLE() do { SBAR(); asm volatile("s_nop 7\n\ts_nop 1" ::: "memory"); SBAR(); } while (0)
DI unsigned rfl(unsigned x) { return __builtin_amdgcn_readfirstlane(x); }
DI unsigned cvtpk(float lo, float hi) { f32x2 v = {lo, hi}; bf16x2_t b = __builtin_convertvector(v, bf16x2_t); return __builtin_bit_cast(unsigned, b); }
DI float bf_lo(unsigned w) { return __uint_as_float(w << 16); }
DI float bf_hi(unsigned w) { return __uint_as_float(w & 0xffff0000u); }
DI rsrc_t mk_rsrc(const void* p, unsigned bytes) { return __builtin_amdgcn_make_buffer_rsrc((void*)p, (short)0, (int)bytes, 0x00020000); }
DI bf16x8 lds_rd128(const LAS char* p) { return *(const LAS bf16x8*)p; }
typedef short v4i16_t __attribute__((ext_vector_type(4)));
DI s16x4 lds_rdtr(const LAS char* p) { return __builtin_bit_cast(s16x4, __builtin_amdgcn_ds_read_tr16_b64_v4i16((LAS v4i16_t*)p)); }
DI float max3f(float a, float b, float c) { float r; asm("v_max3_f32 %0, %1, %2, %3" : "=v"(r) : "v"(a), "v"(b), "v"(c)); return r; }
DI float fadd1(float a, float b) { float r; asm("v_add_f32_e32 %0, %1, %2" : "=v"(r) : "v"(a), "v"(b)); return r; }
DI void set_m0(unsigned a) { asm volatile("s_mov_b32 m0, %0\n\ts_nop 0" ::"s"(a) : "memory"); }
template <int OFF> DI void dma16m(unsigned voff, rsrc_t srd, unsigned soff) {
  asm volatile("buffer_load_dwordx4 %0, %1, %2 offen offset:%c3 lds" ::"v"(voff), "s"(srd), "s"(soff), "i"(OFF) : "memory");
}
DI void dma16(unsigned ldsaddr, unsigned voff, rsrc_t srd, unsigned soff) {
  asm volatile("s_mov_b32 m0, %0\n\ts_nop 0\n\tbuffer_load_dwordx4 %1, %2, %3 offen lds" ::"s"(ldsaddr), "v"(voff), "s"(srd), "s"(soff) : "memory");
}
typedef unsigned srd_t __attribute__((ext_vector_type(4)));
DI srd_t mk_srd(const void* p, unsigned bytes) { const unsigned long long a = (unsigned long long)p; return (srd_t){(unsigned)a, (unsigned)(a >> 32) & 0xffffu, bytes, 0x00020000u}; }
DI void dma16s(unsigned ldsaddr, unsigned voff, srd_t srd, unsigned soff) {
  asm volatile("s_mov_b32 m0, %0\n\ts_nop 0\n\tbuffer_load_dwordx4 %1, %2, %3 offen lds" ::"s"(ldsaddr), "v"(voff), "s"(srd), "s"(soff) : "memory");
}
DI void touch_lds(unsigned ldsaddr, unsigned voff, rsrc_t srd, unsigned soff) {
  asm volatile("s_mov_b32 m0, %0\n\ts_nop 0\n\tbuffer_load_dword %1, %2, %3 offen lds" ::"s"(ldsaddr), "v"(voff), "s"(srd), "s"(soff) : "memory");
}
template <int AUX> DI void st128(const u32x4& w, rsrc_t r, unsigned voff) { asm volatile("" : "+v"(voff)); __builtin_amdgcn_raw_buffer_store_b128(w, r, voff, 0, AUX); }
template <int AUX> DI void st64(const u32x2& w, rsrc_t r, unsigned voff) { asm volatile("" : "+v"(voff)); __builtin_amdgcn_raw_buffer_store_b64(w, r, voff, 0, AUX); }
DI float xrow_max(float x) {
  auto a = __builtin_amdgcn_permlane32_swap(__float_as_uint(x), __float_as_uint(x), false, false); x = fmaxf(__uint_as_float(a[0]), __uint_as_float(a[1]));
  auto b = __builtin_amdgcn_permlane16_swap(__float_as_uint(x), __float_as_uint(x), false, false); return fmaxf(__uint_as_float(b[0]), __uint_as_float(b[1]));
}
DI float xrow_sum(float x) {
  auto a = __builtin_amdgcn_permlane32_swap(__float_as_uint(x), __float_as_uint(x), false, false); x = __uint_as_float(a[0]) + __uint_as_float(a[1]);
  auto b = __builtin_amdgcn_permlane16_swap(__float_as_uint(x), __float_as_uint(x), false, false); return __uint_as_float(b[0]) + __uint_as_float(b[1]);
}
DI u32x4 pair_swap(unsigned x0, unsigned x1, unsigned y0, unsigned y1) {
  auto a = __builtin_amdgcn_permlane16_swap(x0, y0, false, false);
  auto b = __builtin_amdgcn_permlane16_swap(x1, y1, false, false);
  return (u32x4){a[0], b[0], a[1], b[1]};
}

#define NOWARM 1
namespace proj {
constexpr int LDS_X = 0, LDS_W = 65536, LDS_T = 131072, LDS_BYTES = 163840;
constexpr float QSCALE = 0.08838834764831845f * 1.4426950408889634f;

struct Src {
  rsrc_t X, W; unsigned vX[4], vW[2]; unsigned sX, sW, wstep, rowX8;
  unsigned rowW2;
};
struct Frag { bf16x8 x[8]; s16x4 wl[2][4], wh[2][4]; };
#define WF(ks, n) __builtin_shufflevector(fr.wl[ks][n], fr.wh[ks][n], 0, 1, 2, 3, 4, 5, 6, 7)
struct Adr { const LAS char* x[2][2]; const LAS char* w[4]; };

enum { U_Q = 0, U_K = 1, U_V = 2, U_B = 3 };
struct Unit { int kind, pm, pn; };

struct Ptrs { const u16 *Ax, *ckv, *Bx, *wq, *wuk, *wuv, *wqkv; };
DI int fresh_lane() { int l; asm volatile("v_mbcnt_lo_u32_b32 %0, -1, 0\n\tv_mbcnt_hi_u32_b32 %0, -1, %0" : "=v"(l)); return l; }
DI void setup_src(Src& s, const Unit& u, const Ptrs& P, int wid) {
  const int lane = fresh_lane();
  const u16 *Ax = P.Ax, *ckv = P.ckv, *Bx = P.Bx, *wq = P.wq, *wuk = P.wuk, *wuv = P.wuv, *wqkv = P.wqkv;
  const int ldx = (u.kind == U_K || u.kind == U_V) ? 512 : 640;
  const u16* X = u.kind == U_Q ? Ax : (u.kind == U_B ? Bx : ckv);
  s.X = mk_rsrc(X, 2048u * (unsigned)ldx * 2u);
  const int r = lane >> 3, f0 = ((r >> 1) & 1) | (((r >> 2) & 1) << 1);
#pragma unroll
  for (int i = 0; i < 4; ++i) s.vX[i] = (unsigned)(r * ldx * 2) + (unsigned)((((lane & 7) ^ f0 ^ (((i >> 1) & 1) << 1) ^ ((i & 1) << 2)) & 7) * 16);
  s.rowX8 = (unsigned)(8 * ldx * 2);
  s.sX = (unsigned)((u.pm * 256 + wid * 32) * ldx * 2);
  if (u.kind == U_B) {
    s.W = mk_rsrc(wqkv, 640u * 1920u * 2u);
#pragma unroll
    for (int p = 0; p < 2; ++p) { const int sg = ((2 * p + (lane >> 5)) & 3) | ((wid & 1) << 2); const int ch = (lane & 31) ^ (sg << 1);
      const int col = ch < 10 ? u.pn * 160 + ch * 16 : (ch < 20 ? 1280 + u.pn * 160 + (ch - 10) * 16 : (ch < 30 ? 2560 + u.pn * 160 + (ch - 20) * 16 : u.pn * 160));
      s.vW[p] = (unsigned)((lane >> 5) * 3840 + col); }
    s.rowW2 = 2u * 3840u; s.wstep = 64u * 3840u; s.sW = (unsigned)(wid * 8 * 3840);
  } else {
    const u16* W = u.kind == U_Q ? wq : (u.kind == U_K ? wuk : wuv);
    s.W = mk_rsrc(W, (u.kind == U_Q ? 640u : 512u) * 4096u * 2u);
#pragma unroll
    for (int p = 0; p < 2; ++p) { const int sg = ((2 * p + (lane >> 5)) & 3) | ((wid & 1) << 2); s.vW[p] = (unsigned)((lane >> 5) * 8192 + (((lane & 31) ^ (sg << 1)) * 16)); }
    s.rowW2 = 2u * 8192u; s.wstep = 64u * 8192u; s.sW = (unsigned)(wid * 8 * 8192 + u.pn * 512);
  }
}
template <int J> DI void dma_piece(const Src& s, unsigned ldsw, int slot) {
  if constexpr (J < 4) dma16(ldsw + LDS_X + slot * 32768 + J * 1024, s.vX[J], s.X, s.sX + J * s.rowX8);
  else dma16(ldsw + LDS_W + slot * 32768 + (J - 4) * 1024, s.vW[(J - 4) & 1], s.W, s.sW + (J - 4) * s.rowW2);
}
DI void src_next(Src& s) { s.sX += 128u; s.sW += s.wstep; }
struct Warm { rsrc_t r; unsigned voff, soff, step, ldsa; };
DI void setup_warm(Warm& w, const Unit& u, bool on, int tile0, const Ptrs& P, int wid, unsigned ldsa) {
  const int lane = fresh_lane();
  const bool kv = u.kind == U_K || u.kind == U_V;
  const int ldx = kv ? 512 : 640;
  const u16* X = u.kind == U_Q ? P.Ax : (u.kind == U_B ? P.Bx : P.ckv);
  const u16* W = u.kind == U_Q ? P.wq : (u.kind == U_K ? P.wuk : (u.kind == U_V ? P.wuv : P.wqkv));
  const unsigned wbytes = u.kind == U_B ? 640u * 1920u * 2u : (u.kind == U_Q ? 640u : 512u) * 4096u * 2u;
  w.r = mk_rsrc(wid < 4 ? X : W, wid < 4 ? 2048u * (unsigned)ldx * 2u : wbytes);
  if (wid < 4) { const int r = 64 * wid + 2 * (lane & 31) + (u.pn & 1);
    w.voff = (on && lane < 32) ? (unsigned)(r * ldx * 2) : 0x80000000u; w.step = 128u; w.soff = (unsigned)(u.pm * 256 * ldx * 2 + tile0 * 128); }
  else { const int kk = 16 * (wid - 4) + (lane & 15), sh = u.pm & 3;
    if (u.kind == U_B) { const int seg = sh < 3 ? sh : 0; w.voff = (on && lane < 32) ? (unsigned)(kk * 3840 + seg * 1280 + u.pn * 160 + (lane >> 4) * 156) : 0x80000000u; w.step = 64u * 3840u; w.soff = (unsigned)tile0 * w.step; }
    else { w.voff = (on && lane < 16) ? (unsigned)(kk * 8192 + u.pn * 512 + sh * 128) : 0x80000000u; w.step = 64u * 8192u; w.soff = (unsigned)tile0 * w.step; } }
  w.soff = rfl(w.soff); w.step = rfl(w.step); w.ldsa = ldsa;
}
DI void warm(Warm& w) { }

DI void setup_adr(Adr& a, const LAS char* lds, bool kappa, int wr, int wc) {
  const int lane = fresh_lane();
  const int c = lane & 15, g = lane >> 4;
#pragma unroll
  for (int par = 0; par < 2; ++par) {
    const int row = kappa ? 8 * (c >> 2) + 4 * par + (c & 3) : 16 * par + c;
    const int f = ((row >> 1) & 1) | ((((row >> 2) ^ (row >> 4)) & 1) << 1) | (((row >> 3) & 1) << 2);
#pragma unroll
    for (int ks = 0; ks < 2; ++ks) a.x[par][ks] = lds + LDS_X + wr * 16384 + row * 128 + (((4 * ks + g) ^ f) * 16);
  }
  const int q = (lane & 15) >> 2, p = lane & 3, sg = q | ((g & 1) << 2);
#pragma unroll
  for (int n = 0; n < 4; ++n) a.w[n] = lds + LDS_W + (8 * g + q) * 512 + (((4 * wc + n) ^ sg) * 32) + 8 * p;
}
#define BULK_STRIDE 128
DI void bulk_touch(int chunk, const Ptrs& P, const u16* wo, const u16* Bwo, unsigned ldsa, int lane) {
  constexpr unsigned CB = 64u * BULK_STRIDE;
  constexpr unsigned n0 = 2621440u / CB, n1 = n0 + 5242880u / CB, n2 = n1 + 2621440u / CB, n3 = n2 + 2457600u / CB, n4 = n3 + 2097152u / CB, n5 = n4 + 4194304u / CB, n6 = n5 + 4194304u / CB, n7 = n6 + 5242880u / CB, n8 = n7 + 819200u / CB;
  const unsigned c = (unsigned)chunk;
  const u16* p = c < n0 ? P.Ax : c < n1 ? P.wq : c < n2 ? P.Bx : c < n3 ? P.wqkv : c < n4 ? P.ckv : c < n5 ? P.wuk : c < n6 ? P.wuv : c < n7 ? wo : Bwo;
  const unsigned b = c < n0 ? 0u : c < n1 ? n0 : c < n2 ? n1 : c < n3 ? n2 : c < n4 ? n3 : c < n5 ? n4 : c < n6 ? n5 : c < n7 ? n6 : n7;
  touch_lds(ldsa, c < n8 ? (unsigned)lane * BULK_STRIDE : 0x80000000u, mk_rsrc(p, 0x7fffffffu), rfl((c - b) * CB));
}
constexpr int BULK_N = (BULK_STRIDE == 128) ? 2 : 4;
#define RDX(SL, f) (fr.x[(f) & 7] = lds_rd128(ad.x[(f) & 1][((f) >> 3) & 1] + (SL) * 32768 + (((f) & 7) >> 1) * 4096))
#define RDW(SL, ks, n, h) do { if (h) fr.wh[ks][n] = lds_rdtr(ad.w[n] + (SL) * 32768 + (ks) * 16384 + 2048); else fr.wl[ks][n] = lds_rdtr(ad.w[n] + (SL) * 32768 + (ks) * 16384); } while (0)

#define XWAIT(FIRST) do { if (FIRST) asm volatile("s_waitcnt vmcnt(%0) lgkmcnt(0)\n\ts_barrier" ::"i"(BULK_N) : "memory"); else asm volatile("s_waitcnt vmcnt(0) lgkmcnt(0)\n\ts_barrier" ::: "memory"); } while (0)
template <int SL, bool FIRST, bool SWAP>
DI void ktile(f32x4 (&acc)[8][4], Frag& fr, const Adr& ad, Src& src, Warm& wm, const Unit& nu, const Ptrs& P, int wid, bool switch_src, bool do_dma, bool switch_warm, bool has_next, unsigned ldsw) {
#define MM(f, n) do { \
    if (FIRST && (f) < 8) { if (SWAP) mfma_zero(acc[(f) & 7][n], fr.x[(f) & 7], WF(0, n)); else mfma_zero(acc[(f) & 7][n], WF(0, n), fr.x[(f) & 7]); } \
    else { if (SWAP) mfma_acc(acc[(f) & 7][n], fr.x[(f) & 7], WF((f) >> 3, n)); else mfma_acc(acc[(f) & 7][n], WF((f) >> 3, n), fr.x[(f) & 7]); } SBAR(); } while (0)
#define GRP(f) do { \
    if ((f) == 9) { XWAIT(FIRST); SBAR(); if (switch_src) setup_src(src, nu, P, wid); if (switch_warm) setup_warm(wm, nu, has_next, 0, P, wid, wm.ldsa); } \
    MM(f, 0); \
    if ((f) < 4) { RDW(SL, 1, (f) & 3, 0); } else if ((f) >= 9 && (f) < 13) { RDW(SL ^ 1, 0, ((f) - 9) & 3, 0); } \
    SBAR(); \
    MM(f, 1); \
    if ((f) < 4) { RDW(SL, 1, (f) & 3, 1); } else if ((f) >= 9 && (f) < 13) { RDW(SL ^ 1, 0, ((f) - 9) & 3, 1); } \
    SBAR(); \
    MM(f, 2); \
    if (do_dma) { if ((f) == 9) dma_piece<0>(src, ldsw, SL); if ((f) == 10) dma_piece<4>(src, ldsw, SL); if ((f) == 11) dma_piece<1>(src, ldsw, SL); if ((f) == 12) dma_piece<5>(src, ldsw, SL); \
                  if ((f) == 13) dma_piece<2>(src, ldsw, SL); if ((f) == 14) dma_piece<6>(src, ldsw, SL); if ((f) == 15) { dma_piece<3>(src, ldsw, SL); } } \
    SBAR(); \
    MM(f, 3); \
    if ((f) == 15) { if (do_dma) { dma_piece<7>(src, ldsw, SL); src_next(src); } warm(wm); } \
    if ((f) < 8) RDX(SL, (f) + 8); else if ((f) == 9) { RDX(SL ^ 1, 0); SBAR(); RDX(SL ^ 1, 1); } else if ((f) > 9) RDX(SL ^ 1, (f) - 8); \
    SBAR(); } while (0)
  GRP(0); GRP(1); GRP(2); GRP(3); GRP(4); GRP(5); GRP(6); GRP(7); GRP(8); GRP(9); GRP(10); GRP(11); GRP(12); GRP(13); GRP(14); GRP(15);
#undef GRP
#undef MM
}

template <bool SWAP>
DI void run_unit(f32x4 (&acc)[8][4], const Adr& ad, Src& src, Warm& wm, const Unit& nu, const Ptrs& P, int wid, bool has_next, int nt, unsigned ldsw) {
  Frag fr;
  if (SWAP) asm volatile("; unit: tokens on rows" ::: "memory"); else asm volatile("; unit: weights on rows" ::: "memory");
  RDW(0, 0, 0, 0); RDW(0, 0, 0, 1); RDX(0, 0); RDW(0, 0, 1, 0); RDW(0, 0, 1, 1); RDW(0, 0, 2, 0); RDW(0, 0, 2, 1); RDW(0, 0, 3, 0); RDW(0, 0, 3, 1);
  RDX(0, 1); RDX(0, 2); RDX(0, 3); RDX(0, 4); RDX(0, 5); RDX(0, 6); RDX(0, 7);
  SBAR();
  ktile<0, true, SWAP>(acc, fr, ad, src, wm, nu, P, wid, false, true, false, has_next, ldsw);
  ktile<1, false, SWAP>(acc, fr, ad, src, wm, nu, P, wid, false, true, false, has_next, ldsw);
  for (int t = 2; t < nt - 2; t += 2) {
    ktile<0, false, SWAP>(acc, fr, ad, src, wm, nu, P, wid, false, true, t == nt - 4, has_next, ldsw);
    ktile<1, false, SWAP>(acc, fr, ad, src, wm, nu, P, wid, false, true, false, has_next, ldsw);
  }
  ktile<0, false, SWAP>(acc, fr, ad, src, wm, nu, P, wid, has_next, has_next, false, has_next, ldsw);
  ktile<1, false, SWAP>(acc, fr, ad, src, wm, nu, P, wid, false, has_next, false, has_next, ldsw);
  MFMA_SETTLE();
}

#define ST_AUX 0
DI void st16(const u32x4& w, rsrc_t r, unsigned voff, unsigned soff) { __builtin_amdgcn_raw_buffer_store_b128(w, r, voff + soff, 0, ST_AUX); }
DI void epi_qk(const f32x4 (&acc)[8][4], const Unit& u, u16* __restrict__ dst, float scale, int wr, int wc) {
  const int lane = fresh_lane();
  const int g = lane >> 4; const unsigned voff = (unsigned)((lane & 15) + 16 * (((g & 1) << 1) | (g >> 1))) * 16u;
  const rsrc_t r = mk_rsrc(dst, 2048u * 4096u * 2u);
  const unsigned base = (unsigned)(((u.pn * 2 + (wc >> 1)) * 128 + u.pm * 16 + wr * 8) * 4 + (wc & 1) * 2) * 1024u;
#pragma unroll
  for (int m = 0; m < 8; ++m)
#pragma unroll
    for (int t = 0; t < 2; ++t) {
      const f32x4 a = acc[m][2 * t] * scale, b = acc[m][2 * t + 1] * scale;
      st16(pair_swap(cvtpk(a[0], a[1]), cvtpk(a[2], a[3]), cvtpk(b[0], b[1]), cvtpk(b[2], b[3])), r, voff + t * 1024, base + m * 4096);
    }
}
DI void epi_v(const f32x4 (&acc)[8][4], const Unit& u, u16* __restrict__ Vf, int wr, int wc) {
  const unsigned voff = (unsigned)fresh_lane() * 16u;
  const rsrc_t r = mk_rsrc(Vf, 2048u * 4096u * 2u);
  const unsigned base = (unsigned)(((u.pn * 2 + (wc >> 1)) * 64 + u.pm * 8 + wr * 4) * 8 + (wc & 1) * 4) * 1024u;
#pragma unroll
  for (int mm = 0; mm < 4; ++mm)
#pragma unroll
    for (int n = 0; n < 4; ++n) {
      const f32x4 a = acc[2 * mm][n], b = acc[2 * mm + 1][n];
      st16((u32x4){cvtpk(a[0], a[1]), cvtpk(a[2], a[3]), cvtpk(b[0], b[1]), cvtpk(b[2], b[3])}, r, voff + n * 1024, base + mm * 8192);
    }
}
constexpr int T_PITCH = 528, PH_OFF = 256 * T_PITCH, PH_PITCH = 96, PH_WAVE = 32 * PH_PITCH;
static_assert(PH_OFF + 8 * PH_WAVE + 6 * 1024 <= LDS_BYTES + 6 * 1024 && PH_OFF + 8 * PH_WAVE <= LDS_BYTES, "LDS map");
#define BSTAMP(i)
DI void epi_b(const f32x4 (&acc)[8][4], const Unit& u, const LAS char* lds_c, const u16* __restrict__ wphi, u16* __restrict__ PhiQ, float* __restrict__ Part, int wid, unsigned long long* bts) {
  LAS char* lds = (LAS char*)lds_c;
  const int lane = fresh_lane(), c = lane & 15, g = lane >> 4, gp = ((g & 1) << 1) | (g >> 1), wr = wid >> 2, wc = wid & 3;
  const int hh = wid & 3, half = wid >> 2, head = u.pn * 4 + hh;
  asm volatile("s_waitcnt vmcnt(0) lgkmcnt(0)\n\ts_barrier" ::: "memory");
#pragma unroll
  for (int m = 0; m < 8; ++m)
#pragma unroll
    for (int t = 0; t < 2; ++t) { const f32x4 a = acc[m][2 * t], b = acc[m][2 * t + 1];
      *(LAS u32x4*)(lds + (wr * 128 + m * 16 + c) * T_PITCH + (wc * 64 + t * 32 + gp * 8) * 2) = pair_swap(cvtpk(a[0], a[1]), cvtpk(a[2], a[3]), cvtpk(b[0], b[1]), cvtpk(b[2], b[3])); }
  SBAR();
  u16 wraw[16];
#pragma unroll
  for (int j = 0; j < 8; ++j) { const int d = 8 * g + j, dd = d < 20 ? d : 0; wraw[j] = wphi[head * 400 + dd * 20 + c]; wraw[8 + j] = wphi[head * 400 + dd * 20 + 16 + (c & 3)]; }
  bf16x8 w0, w1;
#pragma unroll
  for (int j = 0; j < 8; ++j) { const int d = 8 * g + j; w0[j] = d < 20 ? (short)wraw[j] : (short)0; w1[j] = (d < 20 && c < 4) ? (short)wraw[8 + j] : (short)0; }
  asm volatile("s_waitcnt lgkmcnt(0)\n\ts_barrier" ::: "memory");
  BSTAMP(0);
  LAS char* ph = lds + PH_OFF + wid * PH_WAVE;
  const rsrc_t rq = mk_rsrc(PhiQ, 32u * 128u * 1280u);
  const unsigned vq0 = (unsigned)lane * 16u, vq1 = g == 0 ? 1024u + (unsigned)c * 16u : 0x80000000u;
  f32x4 kv[3][2];
#pragma unroll
  for (int i = 0; i < 3; ++i) { kv[i][0] = (f32x4){}; kv[i][1] = (f32x4){}; }
  const int q4 = c >> 2, p4 = c & 3;
  const unsigned mlo = g == 3 ? 0u : 0xffffffffu, mhi = g >= 2 ? 0u : 0xffffffffu;
  const float NEG = -__builtin_inff(), POS = __builtin_inff();
  constexpr float L2E = 1.4426950408889634f;
  for (int step = 0; step < 4; ++step) {
    const int r0 = half * 128 + step * 32;
#pragma unroll
    for (int b2 = 0; b2 < 2; ++b2) {
    f32x4 a[2], b[2];
#pragma unroll
    for (int i = 0; i < 2; ++i) { const LAS char* xr = lds + (r0 + b2 * 16 + c) * T_PITCH + (i * 80 + hh * 20 + 8 * g) * 2;
      u32x2 lo = *(const LAS u32x2*)xr, hi = *(const LAS u32x2*)(xr + 8); lo &= mlo; hi &= mhi;
      const bf16x8 x = __builtin_bit_cast(bf16x8, (u32x4){lo[0], lo[1], hi[0], hi[1]});
      a[i] = MFMA16(w0, x, ((f32x4){})); b[i] = MFMA16(w1, x, ((f32x4){})); }
    float mx[2], mn[2];
#pragma unroll
    for (int i = 0; i < 2; ++i) {
      const float bx = g == 0 ? fmaxf(fmaxf(b[i][0], b[i][1]), fmaxf(b[i][2], b[i][3])) : NEG, bn = g == 0 ? fminf(fminf(b[i][0], b[i][1]), fminf(b[i][2], b[i][3])) : POS;
      mx[i] = fmaxf(fmaxf(fmaxf(a[i][0], a[i][1]), fmaxf(a[i][2], a[i][3])), bx); mn[i] = -fminf(fminf(fminf(a[i][0], a[i][1]), fminf(a[i][2], a[i][3])), bn); }
#pragma unroll
    for (int i = 0; i < 2; ++i) { mx[i] = xrow_max(mx[i]) * L2E; mn[i] = xrow_max(mn[i]) * L2E; }
    f32x4 p0[2], p1[2], m0[2], m1[2]; float sp[2], sm[2];
#pragma unroll
    for (int i = 0; i < 2; ++i) { const float mxb = g == 0 ? mx[i] : POS, mnb = g == 0 ? mn[i] : POS;
#pragma unroll
      for (int r = 0; r < 4; ++r) { p0[i][r] = __builtin_amdgcn_exp2f(fmaf(a[i][r], L2E, -mx[i])); m0[i][r] = __builtin_amdgcn_exp2f(fmaf(a[i][r], -L2E, -mn[i]));
                                    p1[i][r] = __builtin_amdgcn_exp2f(fmaf(b[i][r], L2E, -mxb)); m1[i][r] = __builtin_amdgcn_exp2f(fmaf(b[i][r], -L2E, -mnb)); }
      sp[i] = ((p0[i][0] + p0[i][1]) + (p0[i][2] + p0[i][3])) + ((p1[i][0] + p1[i][1]) + (p1[i][2] + p1[i][3]));
      sm[i] = ((m0[i][0] + m0[i][1]) + (m0[i][2] + m0[i][3])) + ((m1[i][0] + m1[i][1]) + (m1[i][2] + m1[i][3])); }
#pragma unroll
    for (int i = 0; i < 2; ++i) { sp[i] = __builtin_amdgcn_rcpf(xrow_sum(sp[i])); sm[i] = __builtin_amdgcn_rcpf(xrow_sum(sm[i])); }
#pragma unroll
    for (int i = 0; i < 2; ++i) {
      const u32x4 f0 = {cvtpk(p0[i][0] * sp[i], p0[i][1] * sp[i]), cvtpk(p0[i][2] * sp[i], p0[i][3] * sp[i]), cvtpk(m0[i][0] * sm[i], m0[i][1] * sm[i]), cvtpk(m0[i][2] * sm[i], m0[i][3] * sm[i])};
      const u32x4 f1 = {cvtpk(p1[i][0] * sp[i], p1[i][1] * sp[i]), cvtpk(p1[i][2] * sp[i], p1[i][3] * sp[i]), cvtpk(m1[i][0] * sm[i], m1[i][1] * sm[i]), cvtpk(m1[i][2] * sm[i], m1[i][3] * sm[i])};
      if (i == 0) {
        const unsigned so = (unsigned)(head * 128 + u.pm * 16 + (r0 >> 4) + b2) * 1280u;
        st16(f0, rq, vq0, so); st16(f1, rq, vq1, so);
      } else {
        LAS char* pr = ph + (b2 * 16 + c) * PH_PITCH;
        *(LAS u32x2*)(pr + 8 * g) = (u32x2){f0[0], f0[1]};
        *(LAS u32x2*)(pr + 32 + 8 * g) = (u32x2){f0[2], f0[3]};
        *(LAS u32x4*)(pr + (g == 0 ? 64 : 80)) = f1;
      }
    }
    SBAR();
    }
    asm volatile("s_waitcnt lgkmcnt(0)" ::: "memory");
    bf16x8 vb[2];
#pragma unroll
    for (int db = 0; db < 2; ++db) { const LAS char* av = lds + (r0 + 8 * g + q4) * T_PITCH + (160 + hh * 20 + 16 * db + 4 * p4) * 2;
      vb[db] = __builtin_shufflevector(lds_rdtr(av), lds_rdtr(av + 4 * T_PITCH), 0, 1, 2, 3, 4, 5, 6, 7); }
    if (c == 4) vb[1] = (bf16x8){0x3F80, 0x3F80, 0x3F80, 0x3F80, 0x3F80, 0x3F80, 0x3F80, 0x3F80};
#pragma unroll
    for (int ab = 0; ab < 3; ++ab) { const LAS char* ap = ph + (8 * g + q4) * PH_PITCH + (16 * ab + 4 * p4) * 2;
      const bf16x8 pa = __builtin_shufflevector(lds_rdtr(ap), lds_rdtr(ap + 4 * PH_PITCH), 0, 1, 2, 3, 4, 5, 6, 7);
      kv[ab][0] = MFMA16(pa, vb[0], kv[ab][0]); kv[ab][1] = MFMA16(pa, vb[1], kv[ab][1]); }
    asm volatile("s_waitcnt lgkmcnt(0)" ::: "memory");
  }
  BSTAMP(1);
  asm volatile("s_barrier" ::: "memory");
  LAS char* ex = lds + PH_OFF + hh * 6144 + lane * 16;
  if (half == 1) {
#pragma unroll
    for (int i = 0; i < 6; ++i) *(LAS f32x4*)(ex + i * 1024) = kv[i >> 1][i & 1]; }
  asm volatile("s_waitcnt lgkmcnt(0)\n\ts_barrier" ::: "memory");
  if (half == 0) {
#pragma unroll
    for (int i = 0; i < 6; ++i) ((f32x4*)Part)[(((size_t)u.pm * 32 + head) * 6 + i) * 64 + lane] = kv[i >> 1][i & 1] + *(const LAS f32x4*)(ex + i * 1024); }
}
DI void relayout_w(const u16* __restrict__ wo, const u16* __restrict__ Bwo, u16* __restrict__ WfA, u16* __restrict__ WfB, const LAS char* lds, int wid, int gw, int nw) {
  const int lane = fresh_lane(), g = lane >> 4, q4 = (lane & 15) >> 2, p4 = lane & 3;
  const unsigned ldsw = (unsigned)(uintptr_t)lds + (unsigned)wid * 16384u;
  const rsrc_t rA = mk_rsrc(wo, 4096u * 640u * 2u), rB = mk_rsrc(Bwo, 640u * 640u * 2u);
  const unsigned voff = (unsigned)((lane >> 1) * 1280 + (lane & 1) * 16);
  const LAS char* rd = lds + wid * 16384 + (8 * g + q4) * 32 + 8 * p4;
  for (int it = gw; it < 1480; it += 3 * nw) {
#pragma unroll
    for (int e = 0; e < 3; ++e) { const int x = it + e * nw; const int ks = x / 10, ng = x - ks * 10; const bool isA = ks < 128; const rsrc_t r = isA ? rA : rB;
      const unsigned so = x < 1480 ? (unsigned)((isA ? ks : ks - 128) * 32 * 1280 + ng * 128) : 0u;
#pragma unroll
      for (int n = 0; n < 4; ++n) dma16(ldsw + (unsigned)(e * 4 + n) * 1024u, voff, r, so + n * 32u); }
    asm volatile("s_waitcnt vmcnt(0)" ::: "memory");
#pragma unroll
    for (int e = 0; e < 3; ++e) { const int x = it + e * nw; const int ks = x / 10, ng = x - ks * 10;
#pragma unroll
      for (int n = 0; n < 4; ++n) { const LAS char* a = rd + (e * 4 + n) * 1024;
        const bf16x8 w = __builtin_shufflevector(lds_rdtr(a), lds_rdtr(a + 4 * 32), 0, 1, 2, 3, 4, 5, 6, 7);
        if (x < 1480) { bf16x8* dst = ks < 128 ? (bf16x8*)WfA + (size_t)ks * 40 * 64 : (bf16x8*)WfB + (size_t)(ks - 128) * 40 * 64; dst[(ng * 4 + n) * 64 + lane] = w; } } }
    asm volatile("s_waitcnt lgkmcnt(0)" ::: "memory");
  }
}

__global__ __launch_bounds__(512, 2) void proj_kernel(const u16* __restrict__ Ax, const u16* __restrict__ ckv, const u16* __restrict__ wq, const u16* __restrict__ wuk, const u16* __restrict__ wuv,
                                                      const u16* __restrict__ Bx, const u16* __restrict__ wqkv, u16* __restrict__ Qf, u16* __restrict__ Kf, u16* __restrict__ Vf,
                                                      const u16* __restrict__ wphi, u16* __restrict__ PhiQ, float* __restrict__ Part,
                                                      const u16* __restrict__ wo, const u16* __restrict__ Bwo, u16* __restrict__ WfA, u16* __restrict__ WfB, unsigned* __restrict__ flags, unsigned long long* dbg) {
  extern __shared__ __attribute__((aligned(16))) char smem[];
  const LAS char* lds = (const LAS char*)smem;
  const int tid = threadIdx.x, lane = tid & 63, wid = (int)rfl(tid >> 6), wr = wid >> 2, wc = wid & 3;
  const unsigned ldsw = (unsigned)(uintptr_t)lds + (unsigned)wid * 4096u;
  const int x = blockIdx.x & 7, j = blockIdx.x >> 3;
  auto kv_unit = [&](int k) { Unit u; u.kind = (k & 2) ? U_V : U_K; u.pn = 2 * x + (k & 1); u.pm = k >> 2; return u; };
  Unit u0, u1; bool two = true;
  if (j < 16) { u0.kind = U_Q; u0.pn = 2 * x + (j & 1); u0.pm = j >> 1; u1 = kv_unit(j); }
  else if (j < 24) { u0 = kv_unit(16 + 2 * (j - 16)); u1 = kv_unit(17 + 2 * (j - 16)); }
  else { u0.kind = U_B; u0.pn = x; u0.pm = j - 24; u1 = u0; two = false; }
  const Ptrs P = {Ax, ckv, Bx, wq, wuk, wuv, wqkv};
  Src src;
  setup_src(src, u0, P, wid);
  dma_piece<0>(src, ldsw, 0); dma_piece<4>(src, ldsw, 0); dma_piece<1>(src, ldsw, 0); dma_piece<5>(src, ldsw, 0); dma_piece<2>(src, ldsw, 0); dma_piece<6>(src, ldsw, 0); dma_piece<3>(src, ldsw, 0); dma_piece<7>(src, ldsw, 0); src_next(src);
  dma_piece<0>(src, ldsw, 1); dma_piece<4>(src, ldsw, 1); dma_piece<1>(src, ldsw, 1); dma_piece<5>(src, ldsw, 1); dma_piece<2>(src, ldsw, 1); dma_piece<6>(src, ldsw, 1); dma_piece<3>(src, ldsw, 1); dma_piece<7>(src, ldsw, 1); src_next(src);
  Warm wm; setup_warm(wm, u0, true, 2, P, wid, (unsigned)(uintptr_t)lds + LDS_T + (unsigned)wid * 256u);
  f32x4 acc[8][4]; Adr ad;
  Unit u = u0;
  asm volatile("s_waitcnt vmcnt(8)\n\ts_barrier" ::: "memory");
  SBAR();
#pragma unroll
  for (int i = 0; i < BULK_N; ++i) bulk_touch((int)blockIdx.x * 8 + wid + i * 2048, P, wo, Bwo, (unsigned)(uintptr_t)lds + LDS_T + (unsigned)wid * 256u, lane);

  SBAR();
  for (int ui = 0; ui < 2; ++ui) {
    const bool has_next = two && ui == 0;
    const int nt = (u.kind == U_K || u.kind == U_V) ? 8 : 10;
    setup_adr(ad, lds, u.kind == U_K || u.kind == U_V, wr, wc);
    if (u.kind == U_V) run_unit<true>(acc, ad, src, wm, u1, P, wid, has_next, nt, ldsw);
    else run_unit<false>(acc, ad, src, wm, u1, P, wid, has_next, nt, ldsw);
    if (u.kind == U_V) epi_v(acc, u, Vf, wr, wc);
    else if (u.kind == U_B) {
      unsigned long long bts[4] = {};
      epi_b(acc, u, lds, wphi, PhiQ, Part, wid, bts);
      BSTAMP(2);
    }
    else epi_qk(acc, u, u.kind == U_Q ? Qf : Kf, u.kind == U_Q ? QSCALE : 1.f, wr, wc);
    if (!has_next) break;
    u = u1;
  }
  if (j >= 16 && j < 24) {
    asm volatile("s_waitcnt vmcnt(0) lgkmcnt(0)\n\ts_barrier" ::: "memory");
    relayout_w(wo, Bwo, WfA, WfB, lds, wid, (x * 8 + (j - 16)) * 8 + wid, 512);
    if (blockIdx.x == 128 && wid < 4) flags[(wid * 64 + fresh_lane()) * 16] = 0u;
  }
  asm volatile("s_waitcnt vmcnt(0)" ::: "memory");
}
}

#define MFMASUM 1
namespace attn {
constexpr int LDS_K = 0, LDS_V = 65536, LDS_X = 131072, LDS_BYTES = 163840;
constexpr float L_LIMIT = 1.2676506e30f;

struct Til { f32x4 s[8]; };
struct St {
  f32x4 o[16];
  bf16x8 q[8];
  f32x4 negm[2];
  bf16x8 fr[8];
  u32x4 pw[4];
  f32x2 l[2];
  f32x4 ls[2]; bf16x8 ones;
  float m[2];
};

DI void cmask(Til& t, int thr) {
  const float NEG = -__builtin_inff();
#pragma unroll
  for (int kb = 0; kb < 4; ++kb)
#pragma unroll
    for (int qb = 0; qb < 2; ++qb)
#pragma unroll
      for (int r = 0; r < 4; ++r) if (32 * (kb >> 1) + 4 * (kb & 1) + r - 16 * qb > thr) t.s[2 * kb + qb][r] = NEG;
}

DI void safe_path(const u16* __restrict__ Qf, const u16* __restrict__ Kf, const u16* __restrict__ Vf, u16* __restrict__ Cf, int head, int blk, int rg, int grp, int lane) {
  const int g = lane >> 4, c = lane & 15;
  for (int qb = 0; qb < 2; ++qb) {
    const bf16x8* qp = (const bf16x8*)Qf + ((size_t)(head * 64 + blk * 4 + rg) * 8 + qb * 4) * 64 + lane;
    bf16x8 q[4];
#pragma unroll
    for (int d = 0; d < 4; ++d) q[d] = qp[d * 64];
    f32x4 o[4] = {};
    float m = -1e30f, l = 0.f;
    const int row = blk * 128 + rg * 32 + qb * 16 + c;
    const int nsteps = blk * 4 + rg + 1;
    for (int ks = 0; ks < nsteps; ++ks) {
      const bf16x8* kp = (const bf16x8*)Kf + ((size_t)(head * 32 + (ks >> 1)) * 16 + (ks & 1) * 8) * 64 + lane;
      f32x4 s[2] = {};
      float mx = -__builtin_inff();
#pragma unroll
      for (int b = 0; b < 2; ++b) {
#pragma unroll
        for (int d = 0; d < 4; ++d) s[b] = MFMA16(kp[(b * 4 + d) * 64], q[d], s[b]);
#pragma unroll
        for (int r = 0; r < 4; ++r) { if (ks * 32 + 8 * g + 4 * b + r > row) s[b][r] = -__builtin_inff(); mx = fmaxf(mx, s[b][r]); }
      }
      mx = xrow_max(mx);
      const float mn = fmaxf(m, mx), al = __builtin_amdgcn_exp2f(m - mn); m = mn;
      float ps = 0.f;
#pragma unroll
      for (int b = 0; b < 2; ++b)
#pragma unroll
        for (int r = 0; r < 4; ++r) { s[b][r] = __builtin_amdgcn_exp2f(s[b][r] - mn); ps += s[b][r]; }
      l = l * al + ps;
      const u32x4 w = {cvtpk(s[0][0], s[0][1]), cvtpk(s[0][2], s[0][3]), cvtpk(s[1][0], s[1][1]), cvtpk(s[1][2], s[1][3])};
      const bf16x8* vp = (const bf16x8*)Vf + ((size_t)(head * 32 + (ks >> 1)) * 16 + (ks & 1) * 8 + grp * 4) * 64 + lane;
#pragma unroll
      for (int db = 0; db < 4; ++db) { o[db] *= al; o[db] = MFMA16(vp[db * 64], __builtin_bit_cast(bf16x8, w), o[db]); }
    }
    const float il = 1.f / xrow_sum(l);
    u32x4* cp = (u32x4*)Cf + ((size_t)(head * 4 + grp * 2) * 128 + blk * 8 + rg * 2 + qb) * 64 + (c + 16 * (((g & 1) << 1) | (g >> 1)));
#pragma unroll
    for (int t = 0; t < 2; ++t)
      cp[t * 128 * 64] = pair_swap(cvtpk(o[2 * t][0] * il, o[2 * t][1] * il), cvtpk(o[2 * t][2] * il, o[2 * t][3] * il), cvtpk(o[2 * t + 1][0] * il, o[2 * t + 1][1] * il), cvtpk(o[2 * t + 1][2] * il, o[2 * t + 1][3] * il));
  }
}

#define A_RDK(n) (st.fr[(n) & 7] = lds_rd128(kp + (n) * 1024))
#define A_RDV(n) (st.fr[(n) & 7] = lds_rd128(vp + (n) * 1024))
#define RDK2(a, b) do { A_RDK(a); SBAR(); A_RDK(b); } while (0)
#define RDV2(a, b) do { A_RDV(a); SBAR(); A_RDV(b); } while (0)
#define EXP2(x) __builtin_amdgcn_exp2f(x)

#define SUMS(qb, a, b) do { } while (0)
struct Dma {
  unsigned voff; rsrc_t srdK, srdV; unsigned kdst, vdst, ksoff, vsoff; };
#define DMA_PIECE(d, j) do { if ((j) == 0) set_m0((d).kdst); if ((j) == 4) set_m0((d).vdst); \
    if ((j) < 4) dma16m<((j) & 3) * 1024>((d).voff, (d).srdK, (d).ksoff); else dma16m<((j) & 3) * 1024>((d).voff, (d).srdV, (d).vsoff); } while (0)
#define XBARL() XBARN(0)
#define XBARN(N) do { SBAR(); asm volatile("s_waitcnt vmcnt(" #N ") lgkmcnt(0)\n\ts_barrier" ::: "memory"); SBAR(); } while (0)
#define XBAR() XBARN(0)

struct Ctx {
  const u16 *Qf, *Kf, *Vf; u16* Cf;
  const u16* PhiQ; const float* Part; u16* Of; int pp;
  const LAS char *lds, *kbase, *vbase;
  unsigned lds0, wpiece;
  int lane, wid, grp, rg, head, T1, g;
};
DI void load_q(St& st, const Ctx& c, int blk) {
  const bf16x8* qp = (const bf16x8*)c.Qf + ((size_t)(c.head * 64 + blk * 4 + c.rg) * 8) * 64 + c.lane;
#pragma unroll
  for (int i = 0; i < 8; ++i) st.q[i] = qp[i * 64];
}
#define PIN_Q() asm volatile("" : "+v"(st.q[0]), "+v"(st.q[1]), "+v"(st.q[2]), "+v"(st.q[3]), "+v"(st.q[4]), "+v"(st.q[5]), "+v"(st.q[6]), "+v"(st.q[7]))
DI void dma_advance(Dma& d, const Ctx& c, int gg) {
  d.kdst ^= 32768u; d.vdst ^= 32768u; d.vsoff = d.ksoff;
  d.ksoff = (gg + 2 == c.T1) ? c.wpiece : d.ksoff + 32768u;
}

template <bool FIRST>
DI void phaseA(St& st, Til& C, const Til& P, const LAS char* kp, const LAS char* vp, const Dma& d) {
#define GA(i) do { \
    if (((i) & 3) == 0) { if (FIRST) mfma_zero(C.s[2 * ((i) >> 2)], st.fr[(i) & 7], st.q[0]); else mfma_init(C.s[2 * ((i) >> 2)], st.fr[(i) & 7], st.q[0], st.negm[0]); } \
    else mfma_acc(C.s[2 * ((i) >> 2)], st.fr[(i) & 7], st.q[(i) & 3]); \
    SBAR(); \
    if (!FIRST) {   \
      const float a_ = P.s[2 * (2 * ((i) >> 3) + (((i) & 3) >> 1)) + (((i) >> 2) & 1)][2 * ((i) & 1)], b_ = P.s[2 * (2 * ((i) >> 3) + (((i) & 3) >> 1)) + (((i) >> 2) & 1)][2 * ((i) & 1) + 1]; \
      { unsigned w_ = cvtpk(a_, b_); PIN(w_); st.pw[(i) >> 2][(i) & 3] = w_; } \
      SUMS(((i) >> 2) & 1, a_, b_); SBAR(); } \
    if (((i) & 3) == 0) { if (FIRST) mfma_zero(C.s[2 * ((i) >> 2) + 1], st.fr[(i) & 7], st.q[4]); else mfma_init(C.s[2 * ((i) >> 2) + 1], st.fr[(i) & 7], st.q[4], st.negm[1]); } \
    else mfma_acc(C.s[2 * ((i) >> 2) + 1], st.fr[(i) & 7], st.q[4 + ((i) & 3)]); \
    SBAR(); \
    if (((i) & 1) == 1) { if ((i) < 8) { RDK2(((i) + 8) & 15, ((i) + 7) & 15); } else if (!FIRST) { RDV2(((i) + 8) & 15, ((i) + 7) & 15); } } \
    if (!FIRST) { if ((i) == 0) DMA_PIECE(d, 3); if ((i) == 2) DMA_PIECE(d, 4); if ((i) == 4) DMA_PIECE(d, 5); if ((i) == 8) DMA_PIECE(d, 6); if ((i) == 10) DMA_PIECE(d, 7); } \
    SBAR(); } while (0)
  GA(0); GA(1); GA(2); GA(3); GA(4); GA(5); GA(6); GA(7); GA(8); GA(9); GA(10); GA(11); GA(12); GA(13); GA(14); GA(15);
#undef GA
}

#define MSUM(i) do { if (((i) & 7) == 2) mfma_acc(st.ls[0], st.ones, __builtin_bit_cast(bf16x8, st.pw[2 * ((i) >> 3)])); if (((i) & 7) == 5) mfma_acc(st.ls[1], st.ones, __builtin_bit_cast(bf16x8, st.pw[2 * ((i) >> 3) + 1])); } while (0)
template <class CTX> DI void phaseB(St& st, Til& C, const LAS char* vp, const LAS char* kn, Dma& d, const CTX& c, int g) {
#define GB(i) do { \
    if ((i) == 11) { XBARL(); dma_advance(d, c, g); } \
    mfma_acc(st.o[2 * ((i) & 7)], st.fr[(i) & 7], __builtin_bit_cast(bf16x8, st.pw[2 * ((i) >> 3)])); \
    SBAR(); \
    { float e_ = EXP2(C.s[(i) >> 1][2 * ((i) & 1)]); PIN(e_); C.s[(i) >> 1][2 * ((i) & 1)] = e_; } \
    SBAR(); \
    mfma_acc(st.o[2 * ((i) & 7) + 1], st.fr[(i) & 7], __builtin_bit_cast(bf16x8, st.pw[2 * ((i) >> 3) + 1])); \
    SBAR(); \
    { float e_ = EXP2(C.s[(i) >> 1][2 * ((i) & 1) + 1]); PIN(e_); C.s[(i) >> 1][2 * ((i) & 1) + 1] = e_; } \
    SBAR(); \
    MSUM(i); \
    if (((i) & 1) == 1 && (i) < 8) { RDV2(((i) + 8) & 15, ((i) + 7) & 15); } \
    if ((i) == 11) { { const LAS char* kp = kn; RDK2(1, 0); SBAR(); RDK2(3, 2); } } \
    if ((i) == 12) { DMA_PIECE(d, 0); } \
    if ((i) == 13) { { const LAS char* kp = kn; RDK2(5, 4); } } \
    if ((i) == 14) { DMA_PIECE(d, 1); DMA_PIECE(d, 2); } \
    if ((i) == 15) { { const LAS char* kp = kn; RDK2(7, 6); } } \
    SBAR(); } while (0)
  GB(0); GB(1); GB(2); GB(3); GB(4); GB(5); GB(6); GB(7); GB(8); GB(9); GB(10); GB(11); GB(12); GB(13); GB(14); GB(15);
#undef GB
}

struct BIn { f32x4 part[8]; bf16x8 q0[2], q1[2]; };
DI void b2_load(BIn& b, const u16* __restrict__ PhiQ, const float* __restrict__ Part, int head, int pp, int wid, int lane) {
  const int c = lane & 15, g = lane >> 4;
  if (wid < 6) {
#pragma unroll
    for (int pm = 0; pm < 8; ++pm) b.part[pm] = ((const f32x4*)Part)[(((size_t)pm * 32 + head) * 6 + wid) * 64 + lane];
  }
#pragma unroll
  for (int i = 0; i < 2; ++i) { const char* src = (const char*)PhiQ + ((size_t)head * 128 + pp * 16 + wid * 2 + i) * 1280;
    b.q0[i] = *(const bf16x8*)(src + lane * 16);
    b.q1[i] = *(const bf16x8*)(src + 1024 + c * 16);
    if (g != 0) b.q1[i] = (bf16x8){0, 0, 0, 0, 0, 0, 0, 0}; }
}
DI void b2_publish(const BIn& b, LAS char* xr, int wid, int lane) {
  if (wid < 6) { const f32x4 s = ((b.part[0] + b.part[1]) + (b.part[2] + b.part[3])) + ((b.part[4] + b.part[5]) + (b.part[6] + b.part[7]));
    *(LAS u32x2*)(xr + wid * 512 + lane * 8) = (u32x2){cvtpk(s[0], s[1]), cvtpk(s[2], s[3])}; }
}
DI void b2_finish(const BIn& b, const LAS char* xr, u16* __restrict__ Of, int head, int pp, int wid, int lane) {
  const int c = lane & 15, g = lane >> 4;
  bf16x8 a0[2], a1[2];
#pragma unroll
  for (int db = 0; db < 2; ++db) {
    a0[db] = __builtin_shufflevector(*(const LAS s16x4*)(xr + db * 512 + lane * 8), *(const LAS s16x4*)(xr + (2 + db) * 512 + lane * 8), 0, 1, 2, 3, 4, 5, 6, 7);
    a1[db] = __builtin_shufflevector(*(const LAS s16x4*)(xr + (4 + db) * 512 + c * 8), *(const LAS s16x4*)(xr + (4 + db) * 512 + (c + 16) * 8), 0, 1, 2, 3, 4, 5, 6, 7);
    if (g != 0) a1[db] = (bf16x8){0, 0, 0, 0, 0, 0, 0, 0};
  }
#pragma unroll
  for (int i = 0; i < 2; ++i) {
    f32x4 n0 = MFMA16(a0[0], b.q0[i], ((f32x4){})); n0 = MFMA16(a1[0], b.q1[i], n0);
    f32x4 n1 = MFMA16(a0[1], b.q0[i], ((f32x4){})); n1 = MFMA16(a1[1], b.q1[i], n1);
    const float inv = 1.f / (__shfl(n1[0], c + 16) + 1e-6f);
    char* base = (char*)Of + (size_t)(pp * 16 + wid * 2 + i) * 1024;
    { const int k = head * 20 + 4 * g; *(u32x2*)(base + (size_t)(k >> 5) * 131072 + (c + 16 * ((k >> 3) & 3)) * 16 + 8 * ((k >> 2) & 1)) = (u32x2){cvtpk(n0[0] * inv, n0[1] * inv), cvtpk(n0[2] * inv, n0[3] * inv)}; }
    if (g == 0) { const int k = head * 20 + 16; *(u32x2*)(base + (size_t)(k >> 5) * 131072 + (c + 16 * ((k >> 3) & 3)) * 16 + 8 * ((k >> 2) & 1)) = (u32x2){cvtpk(n1[0] * inv, n1[1] * inv), cvtpk(n1[2] * inv, n1[3] * inv)}; }
  }
}

template <int BI>
DI void do_block(St& st, Dma& d, Ctx& c, const int blk, const int next_blk) {
  const int T = blk + 1, lane = c.lane, grp = c.grp, rg = c.rg, wid = c.wid;
  int g = c.g;
  const int thr = rg * 32 + (lane & 15) - grp * 64 - 8 * (lane >> 4);
  Til a, b;
  { const LAS char* kp = c.kbase + (g & 1) * 32768;
    A_RDK(1); A_RDK(0); A_RDK(3); A_RDK(2); A_RDK(5); A_RDK(4); A_RDK(7); A_RDK(6);
    phaseA<true>(st, a, a, kp, kp, d);
    MFMA_SETTLE();
    if (T == 1) cmask(a, thr);
#pragma unroll
    for (int qb = 0; qb < 2; ++qb) {
      float mx = max3f(a.s[qb][0], a.s[qb][1], a.s[qb][2]), my = max3f(a.s[qb][3], a.s[2 + qb][0], a.s[2 + qb][1]);
      mx = max3f(mx, a.s[2 + qb][2], a.s[2 + qb][3]); my = max3f(my, a.s[4 + qb][0], a.s[4 + qb][1]);
      mx = max3f(mx, a.s[4 + qb][2], a.s[4 + qb][3]); my = max3f(my, a.s[6 + qb][0], a.s[6 + qb][1]);
      mx = max3f(mx, a.s[6 + qb][2], a.s[6 + qb][3]); mx = max3f(mx, my, -1e30f);
      st.m[qb] = xrow_max(mx); st.l[qb] = (f32x2){0.f, 0.f}; st.ls[qb] = (f32x4){};
    }
    XBAR();
    dma_advance(d, c, g);
    DMA_PIECE(d, 0); DMA_PIECE(d, 1); DMA_PIECE(d, 2);
    { const LAS char* kp = c.kbase + ((g + 1) & 1) * 32768; A_RDK(1); A_RDK(0); A_RDK(3); A_RDK(2); A_RDK(5); A_RDK(4); A_RDK(7); A_RDK(6); }
#pragma unroll
    for (int qb = 0; qb < 2; ++qb) { st.negm[qb] = (f32x4){-st.m[qb], -st.m[qb], -st.m[qb], -st.m[qb]};
#pragma unroll
      for (int kb = 0; kb < 4; ++kb)
#pragma unroll
        for (int r = 0; r < 4; ++r) a.s[2 * kb + qb][r] = EXP2(a.s[2 * kb + qb][r] - st.m[qb]); }
#pragma unroll
    for (int i = 0; i < 16; ++i) st.o[i] = (f32x4){};
    asm volatile("" : "+v"(st.o[0]), "+v"(st.o[1]), "+v"(st.o[2]), "+v"(st.o[3]), "+v"(st.o[4]), "+v"(st.o[5]), "+v"(st.o[6]), "+v"(st.o[7]));
    asm volatile("" : "+v"(st.o[8]), "+v"(st.o[9]), "+v"(st.o[10]), "+v"(st.o[11]), "+v"(st.o[12]), "+v"(st.o[13]), "+v"(st.o[14]), "+v"(st.o[15]));
    asm volatile("s_nop 1" : "+v"(st.negm[0]), "+v"(st.negm[1]), "+v"(st.ls[0]), "+v"(st.ls[1]));
    ++g;
  }
#define STEP(C, P, t, kp_, vp_, kn_) do { \
    phaseA<false>(st, C, P, kp_, vp_, d); \
    if ((t) == T - 1) { MFMA_SETTLE(); cmask(C, thr); } \
    SBAR(); \
    phaseB(st, C, vp_, kn_, d, c, g); \
    ++g; } while (0)
  int t = 1;
  const LAS char* kE = c.kbase + (g & 1) * 32768; const LAS char* kO = c.kbase + ((g + 1) & 1) * 32768;
  const LAS char* vE = c.vbase + ((g + 1) & 1) * 32768; const LAS char* vO = c.vbase + (g & 1) * 32768;
  for (; t + 1 < T; t += 2) { STEP(b, a, t, kE, vE, kO); STEP(a, b, t + 1, kO, vO, kE); }
  if (t < T) { STEP(b, a, t, kE, vE, kO); a = b; }
#undef STEP
  DMA_PIECE(d, 3); DMA_PIECE(d, 4); DMA_PIECE(d, 5); DMA_PIECE(d, 6); DMA_PIECE(d, 7);
  if (BI == 0) load_q(st, c, next_blk);
  BIn bi;
  if (BI == 1) b2_load(bi, c.PhiQ, c.Part, c.head, c.pp, wid, lane);
  { const LAS char* vp = c.vbase + ((g - 1) & 1) * 32768;
    A_RDV(1); A_RDV(0); A_RDV(3); A_RDV(2); A_RDV(5); A_RDV(4); A_RDV(7); A_RDV(6);
#pragma unroll
    for (int i = 0; i < 16; ++i) { const float x = a.s[2 * (2 * (i >> 3) + ((i & 3) >> 1)) + ((i >> 2) & 1)][2 * (i & 1)], y = a.s[2 * (2 * (i >> 3) + ((i & 3) >> 1)) + ((i >> 2) & 1)][2 * (i & 1) + 1];
      st.pw[i >> 2][i & 3] = cvtpk(x, y);
    }
#define GD(i) do { mfma_acc(st.o[2 * ((i) & 7)], st.fr[(i) & 7], __builtin_bit_cast(bf16x8, st.pw[2 * ((i) >> 3)])); \
      mfma_acc(st.o[2 * ((i) & 7) + 1], st.fr[(i) & 7], __builtin_bit_cast(bf16x8, st.pw[2 * ((i) >> 3) + 1])); MSUM(i); \
      if (((i) & 1) == 1 && (i) < 8) { RDV2(((i) + 8) & 15, ((i) + 7) & 15); } SBAR(); } while (0)
    asm volatile("s_nop 1" : "+v"(st.pw[0]), "+v"(st.pw[1]), "+v"(st.pw[2]), "+v"(st.pw[3]));
    GD(0); GD(1); GD(2); GD(3); GD(4); GD(5); GD(6); GD(7); GD(8); GD(9); GD(10); GD(11); GD(12); GD(13); GD(14); GD(15);
    MFMA_SETTLE();
#undef GD
  }
  float l[2] = {st.ls[0][0], st.ls[1][0]};
  LAS char* xr = (LAS char*)c.lds + LDS_X;
  *(LAS f32x4*)(xr + wid * 1024 + lane * 16) = (f32x4){st.m[0], l[0], st.m[1], l[1]};
  asm volatile("s_waitcnt lgkmcnt(0)\n\ts_barrier" ::: "memory");
  const f32x4 oth = *(const LAS f32x4*)(xr + (wid ^ 4) * 1024 + lane * 16);
  bool bad = false; float fs[2];
#pragma unroll
  for (int qb = 0; qb < 2; ++qb) {
    const float M = fmaxf(st.m[qb], oth[2 * qb]);
    const float fo = EXP2(st.m[qb] - M), fp = EXP2(oth[2 * qb] - M);
    const float lt = l[qb] * fo + oth[2 * qb + 1] * fp;
    bad = bad || !(l[qb] < L_LIMIT) || !(oth[2 * qb + 1] < L_LIMIT);
    fs[qb] = fo * __builtin_amdgcn_rcpf(lt);
  }
  LAS char* vslot = (LAS char*)c.lds + LDS_V + ((g - 1) & 1) * 32768;
  LAS char* sendp = vslot + wid * 4096 + lane * 16;
  const LAS char* recvp = vslot + (wid ^ 4) * 4096 + lane * 16;
  f32x4 k[8];
#define PACK8(x, y, f) (u32x4){cvtpk((x)[0] * (f), (x)[1] * (f)), cvtpk((x)[2] * (f), (x)[3] * (f)), cvtpk((y)[0] * (f), (y)[1] * (f)), cvtpk((y)[2] * (f), (y)[3] * (f))}
  if (grp == 0) {
#pragma unroll
    for (int i = 0; i < 4; ++i) *(LAS u32x4*)(sendp + i * 1024) = PACK8(st.o[8 + 4 * (i >> 1) + (i & 1)], st.o[8 + 4 * (i >> 1) + 2 + (i & 1)], fs[i & 1]);
#pragma unroll
    for (int i = 0; i < 8; ++i) k[i] = st.o[i];
  } else {
#pragma unroll
    for (int i = 0; i < 4; ++i) *(LAS u32x4*)(sendp + i * 1024) = PACK8(st.o[4 * (i >> 1) + (i & 1)], st.o[4 * (i >> 1) + 2 + (i & 1)], fs[i & 1]);
#pragma unroll
    for (int i = 0; i < 8; ++i) k[i] = st.o[8 + i];
  }
  asm volatile("s_waitcnt lgkmcnt(0)\n\ts_barrier" ::: "memory");
  float amax = 0.f;
  u32x4 res[4];
#pragma unroll
  for (int i = 0; i < 4; ++i) {
    const u32x4 x = *(const LAS u32x4*)(recvp + i * 1024);
    const f32x4 p = k[4 * (i >> 1) + (i & 1)], q = k[4 * (i >> 1) + 2 + (i & 1)]; const float f = fs[i & 1];
    const float v0 = p[0] * f + bf_lo(x[0]), v1 = p[1] * f + bf_hi(x[0]), v2 = p[2] * f + bf_lo(x[1]), v3 = p[3] * f + bf_hi(x[1]);
    const float v4 = q[0] * f + bf_lo(x[2]), v5 = q[1] * f + bf_hi(x[2]), v6 = q[2] * f + bf_lo(x[3]), v7 = q[3] * f + bf_hi(x[3]);
    amax = max3f(amax, __builtin_fabsf(v0), __builtin_fabsf(v1)); amax = max3f(amax, __builtin_fabsf(v2), __builtin_fabsf(v3));
    amax = max3f(amax, __builtin_fabsf(v4), __builtin_fabsf(v5)); amax = max3f(amax, __builtin_fabsf(v6), __builtin_fabsf(v7));
    res[i] = pair_swap(cvtpk(v0, v1), cvtpk(v2, v3), cvtpk(v4, v5), cvtpk(v6, v7));
  }
  bad = bad || !(amax < 3.0e38f);
  if (__builtin_expect(__any(bad), 0)) {
    safe_path(c.Qf, c.Kf, c.Vf, c.Cf, c.head, blk, rg, grp, lane);
  } else {
#pragma unroll
    for (int i = 0; i < 4; ++i) ((u32x4*)c.Cf)[((size_t)(c.head * 4 + grp * 2 + (i >> 1)) * 128 + blk * 8 + rg * 2 + (i & 1)) * 64 + ((lane & 15) + 16 * ((((lane >> 4) & 1) << 1) | (lane >> 5)))] = res[i];
  }
  if (BI == 1) {
    b2_publish(bi, xr + 16384, wid, lane);
    asm volatile("s_waitcnt lgkmcnt(0)\n\ts_barrier" ::: "memory");
    b2_finish(bi, c.lds + LDS_X + 16384, c.Of, c.head, c.pp, wid, lane);
  }
  c.g = g;
}

__global__ __launch_bounds__(512, 2) void attn_kernel(const u16* __restrict__ Qf, const u16* __restrict__ Kf, const u16* __restrict__ Vf, u16* __restrict__ Cf,
                                                      const u16* __restrict__ PhiQ, const float* __restrict__ Part, u16* __restrict__ Of, unsigned long long* dbg) {
  extern __shared__ __attribute__((aligned(16))) char smem[];
  Ctx c; c.Qf = Qf; c.Kf = Kf; c.Vf = Vf; c.Cf = Cf; c.PhiQ = PhiQ; c.Part = Part; c.Of = Of;
  c.lds = (const LAS char*)smem; c.lds0 = (unsigned)(uintptr_t)c.lds;
  const int tid = threadIdx.x; c.lane = tid & 63;
  c.wid = (int)rfl(tid >> 6); c.grp = c.wid >> 2; c.rg = c.wid & 3;
  const int xcd = blockIdx.x & 7, jj = blockIdx.x >> 3, pp = jj & 7; c.head = xcd * 4 + (jj >> 3);
  c.T1 = 16 - pp; c.g = 0; c.pp = pp;
  c.wpiece = (unsigned)c.wid * 4096u;
  c.kbase = c.lds + LDS_K + c.grp * 16384 + c.lane * 16; c.vbase = c.lds + LDS_V + c.grp * 16384 + c.lane * 16;
  Dma d;
  d.voff = (unsigned)c.lane * 16u;
  d.srdK = mk_rsrc(Kf + (size_t)c.head * 262144, 524288u); d.srdV = mk_rsrc(Vf + (size_t)c.head * 262144, 524288u);
  St st;
  st.ones = (bf16x8){0x3F80, 0x3F80, 0x3F80, 0x3F80, 0x3F80, 0x3F80, 0x3F80, 0x3F80}; PIN(st.ones);
  d.kdst = c.lds0 + LDS_K + c.wpiece; d.ksoff = c.wpiece; d.vdst = c.lds0 + LDS_V + c.wpiece; d.vsoff = c.wpiece;
  DMA_PIECE(d, 0); DMA_PIECE(d, 1); DMA_PIECE(d, 2); DMA_PIECE(d, 3);
  load_q(st, c, 15 - pp);
  PIN_Q();
  XBAR();
  DMA_PIECE(d, 4); DMA_PIECE(d, 5); DMA_PIECE(d, 6); DMA_PIECE(d, 7);
  d.kdst = c.lds0 + LDS_K + 32768 + c.wpiece; d.ksoff = 32768u + c.wpiece;
  DMA_PIECE(d, 0); DMA_PIECE(d, 1); DMA_PIECE(d, 2); DMA_PIECE(d, 3);
  do_block<0>(st, d, c, 15 - pp, pp);
  PIN_Q();
  do_block<1>(st, d, c, pp, pp);
  asm volatile("s_waitcnt vmcnt(0)" ::: "memory");
}
}

namespace outp {
constexpr int SLOTB = 40960, LDS_BYTES = 4 * SLOTB;
struct Src { rsrc_t X, W; unsigned sX, sW; };
struct XSet { bf16x8 f[2][2]; };
struct WSet { bf16x8 f[10]; };
DI void ldx(bf16x8& d, unsigned voff, rsrc_t r, unsigned soff) { asm volatile("buffer_load_dwordx4 %0, %1, %2, %3 offen" : "=v"(d) : "v"(voff), "s"(r), "s"(soff) : "memory"); }
DI void ldx1k(bf16x8& d, unsigned voff, rsrc_t r, unsigned soff) { asm volatile("buffer_load_dwordx4 %0, %1, %2, %3 offen offset:1024" : "=v"(d) : "v"(voff), "s"(r), "s"(soff) : "memory"); }
#define RW(set, p, ks, n) (set.f[n] = lds_rd128((p) + (ks) * 10240 + (n) * 1024))
template <int I> DI void wp(const Src& s, unsigned ldsw, unsigned voff) {
  if constexpr (I == 0 || I == 2) set_m0(ldsw);
  if constexpr (I == 4) set_m0(ldsw + 4096);
  dma16m<(I & 3) * 1024>(voff, s.W, s.sW + (I >> 2) * 4096u);
}
DI void round(f32x4 (&acc)[2][10], XSet& X, WSet& wA, WSet& wB, const LAS char* wc, const LAS char* wn_, Src& src, const Src& srcA, unsigned lw2, unsigned lw3, unsigned voff,
              bool h2, bool h3, int wait, bool swx, bool sww) {
#define MM(k, W_, m, n) do { mfma_acc(acc[m][n], W_.f[n], X.f[k][m]); SBAR(); } while (0)
#define HX(k, m) do { if (h2) { if (m) ldx1k(X.f[k][1], voff, src.X, src.sX + (k) * 262144u); else ldx(X.f[k][0], voff, src.X, src.sX + (k) * 262144u); } SBAR(); } while (0)
#define HW2(i) do { if (h2) wp<i>(src, lw2, voff); SBAR(); } while (0)
#define HW3(i) do { if (h3) wp<i>(src, lw3, voff); SBAR(); } while (0)
#define NOP_() do { } while (0)
#define KSTEP(k, CUR, NXT, p, A, B, C, D) do { \
    MM(k, CUR, 0, 0); RW(NXT, p, 0, 0); SBAR(); MM(k, CUR, 1, 0); MM(k, CUR, 0, 1); RW(NXT, p, 0, 1); SBAR(); MM(k, CUR, 1, 1); A; MM(k, CUR, 0, 2); RW(NXT, p, 0, 2); SBAR(); MM(k, CUR, 1, 2); \
    MM(k, CUR, 0, 3); RW(NXT, p, 0, 3); SBAR(); MM(k, CUR, 1, 3); B; MM(k, CUR, 0, 4); RW(NXT, p, 0, 4); SBAR(); MM(k, CUR, 1, 4); MM(k, CUR, 0, 5); RW(NXT, p, 0, 5); SBAR(); MM(k, CUR, 1, 5); C; \
    MM(k, CUR, 0, 6); RW(NXT, p, 0, 6); SBAR(); MM(k, CUR, 1, 6); MM(k, CUR, 0, 7); RW(NXT, p, 0, 7); SBAR(); MM(k, CUR, 1, 7); D; MM(k, CUR, 0, 8); RW(NXT, p, 0, 8); SBAR(); MM(k, CUR, 1, 8); \
    MM(k, CUR, 0, 9); RW(NXT, p, 0, 9); SBAR(); MM(k, CUR, 1, 9); } while (0)
  if (swx) src.sX = srcA.sX, src.X = srcA.X;
  KSTEP(0, wA, wB, wc + 20480, HW2(2), HW2(3), HW2(4), NOP_());
  HX(0, 0);
  if (h2) src.sW += 4u * 40960u;
  if (wait == 8) asm volatile("s_waitcnt vmcnt(8) lgkmcnt(0)\n\ts_barrier" ::: "memory");
  else if (wait == 6) asm volatile("s_waitcnt vmcnt(6) lgkmcnt(0)\n\ts_barrier" ::: "memory");
  else asm volatile("s_waitcnt vmcnt(0) lgkmcnt(0)\n\ts_barrier" ::: "memory");
  SBAR();
  if (sww) src.sW = srcA.sW, src.W = srcA.W;
  KSTEP(1, wB, wA, wn_, HX(0, 1), HW3(0), HW3(1), NOP_());
  HX(1, 0); HX(1, 1);
  if (h2) src.sX += 4u * 131072u;
#undef NOP_
#undef HW3
#undef HW2
#undef HX
#undef KSTEP
#undef MM
}
#define PIN_X(XS) asm volatile("" : "+v"(XS.f[0][0]), "+v"(XS.f[0][1]), "+v"(XS.f[1][0]), "+v"(XS.f[1][1]))
#define PIN_ACC() do { asm volatile("" : "+v"(acc[0][0]), "+v"(acc[0][1]), "+v"(acc[0][2]), "+v"(acc[0][3]), "+v"(acc[0][4]), "+v"(acc[0][5]), "+v"(acc[0][6]), "+v"(acc[0][7]), "+v"(acc[0][8]), "+v"(acc[0][9])); \
    asm volatile("s_nop 1" : "+v"(acc[1][0]), "+v"(acc[1][1]), "+v"(acc[1][2]), "+v"(acc[1][3]), "+v"(acc[1][4]), "+v"(acc[1][5]), "+v"(acc[1][6]), "+v"(acc[1][7]), "+v"(acc[1][8]), "+v"(acc[1][9])); SBAR(); } while (0)
#define RLX_AGENT __ATOMIC_RELAXED, __HIP_MEMORY_SCOPE_AGENT
DI u32x2 pack4(const f32x4& a) { return (u32x2){cvtpk(a[0], a[1]), cvtpk(a[2], a[3])}; }
DI f32x4 unpack4(unsigned x, unsigned y) { return (f32x4){bf_lo(x), bf_hi(x), bf_lo(y), bf_hi(y)}; }

__global__ __launch_bounds__(512, 2) void outp_kernel(const u16* __restrict__ Cf, const u16* __restrict__ Of, const u16* __restrict__ WfA, const u16* __restrict__ WfB,
                                                      u16* __restrict__ out, u16* __restrict__ Ex, unsigned* __restrict__ flags, unsigned long long* dbg) {
#define OST(i)
  extern __shared__ __attribute__((aligned(16))) char smem[];
  const LAS char* lds = (const LAS char*)smem;
  const int tid = threadIdx.x, lane = tid & 63, wid = (int)rfl(tid >> 6), par = wid >> 2, wm = wid & 3;
  const int x = blockIdx.x & 7, j = blockIdx.x >> 3, kq = x >> 1, pm = 8 * (x & 1) + (j >> 2), pn = j & 3, tile = pm * 4 + pn;
  const unsigned ldsw = (unsigned)(uintptr_t)lds + (unsigned)wid * 5120u, voff = (unsigned)lane * 16u;
  const int a0 = kq == 0 ? 0 : (kq == 1 ? 3 : (kq == 2 ? 13 : 23)), R = kq == 0 ? 8 : (kq == 3 ? 9 : 10);
  const unsigned ox = (unsigned)(par * 128 + pm * 8 + wm * 2) * 1024u, ow = (unsigned)((wid >> 1) * 40 + pn * 10 + (wid & 1) * 5) * 1024u;
  Src srcA, src;
  srcA.X = mk_rsrc(Cf, 2048u * 4096u * 2u); srcA.W = mk_rsrc(WfA, 4096u * 640u * 2u);
  srcA.sX = (unsigned)(a0 * 4 * 128) * 1024u + ox; srcA.sW = (unsigned)(a0 * 4 * 40) * 1024u + ow;
  src.X = mk_rsrc(kq == 0 ? Of : Cf, kq == 0 ? 2048u * 640u * 2u : 2048u * 4096u * 2u); src.W = mk_rsrc(kq == 0 ? WfB : WfA, kq == 0 ? 640u * 640u * 2u : 4096u * 640u * 2u);
  src.sX = kq == 0 ? ox : srcA.sX; src.sW = kq == 0 ? ow : srcA.sW;
  XSet X0, X1;
#define FETCH(XS, slot) do { _Pragma("unroll") for (int k = 0; k < 2; ++k) { ldx(XS.f[k][0], voff, src.X, src.sX + k * 262144u); ldx1k(XS.f[k][1], voff, src.X, src.sX + k * 262144u); } \
    wp<0>(src, ldsw + (slot) * SLOTB, voff); wp<1>(src, ldsw + (slot) * SLOTB, voff); wp<2>(src, ldsw + (slot) * SLOTB, voff); wp<3>(src, ldsw + (slot) * SLOTB, voff); wp<4>(src, ldsw + (slot) * SLOTB, voff); \
    src.sX += 4u * 131072u; src.sW += 4u * 40960u; } while (0)
  FETCH(X0, 0); FETCH(X1, 1);
#undef FETCH
  wp<0>(src, ldsw + 2 * SLOTB, voff); wp<1>(src, ldsw + 2 * SLOTB, voff);
  const LAS char* wa = lds + par * 10240 + lane * 16;
  f32x4 acc[2][10];
#pragma unroll
  for (int m = 0; m < 2; ++m)
#pragma unroll
    for (int n = 0; n < 10; ++n) acc[m][n] = (f32x4){};
  PIN_ACC();
  WSet wA, wB;
  asm volatile("s_waitcnt vmcnt(11)\n\ts_barrier" ::: "memory"); SBAR();
  OST(0);
#pragma unroll
  for (int n = 0; n < 10; ++n) RW(wA, wa, 0, n);
  const int c = lane & 15, g = lane >> 4;
  const rsrc_t rout = mk_rsrc(out, 2048u * 1280u * 2u);
  const unsigned vout = (unsigned)((pm * 128 + wm * 32 + par * 16 + c) * 2560 + (pn * 160 + 4 * g) * 2);
  u32x2 bres[10];
#pragma unroll
  for (int t = 0; t < 10; ++t) bres[t] = (u32x2){0u, 0u};
  unsigned o0 = 0, o1 = SLOTB, o2 = 2 * SLOTB;
  for (int s = 0; s < R; s += 2) {
    PIN_X(X0);
    round(acc, X0, wA, wB, wa + o0, wa + o1, src, srcA, ldsw + o2, ldsw + o0, voff, s + 2 < R, s + 3 < R, s == 0 ? 6 : (s + 2 < R ? 8 : 0), false, kq == 0 && s == 2);
    if (kq == 0 && s == 4) {
      MFMA_SETTLE();
      LAS char* mw = (LAS char*)lds + 3 * SLOTB + wid * 5120 + lane * 8; const LAS char* mr = lds + 3 * SLOTB + (wid ^ 4) * 5120 + lane * 8;
#pragma unroll
      for (int n = 0; n < 10; ++n) *(LAS u32x2*)(mw + n * 512) = pack4(par == 0 ? acc[1][n] : acc[0][n]);
      asm volatile("s_waitcnt lgkmcnt(0)\n\ts_barrier" ::: "memory");
#pragma unroll
      for (int n = 0; n < 10; ++n) { const u32x2 o = *(const LAS u32x2*)(mr + n * 512); bres[n] = pack4((par == 0 ? acc[0][n] : acc[1][n]) + unpack4(o[0], o[1])); acc[0][n] = (f32x4){}; acc[1][n] = (f32x4){}; }
      PIN_ACC();
    }
    if (s + 1 < R) {
      PIN_X(X1);
      round(acc, X1, wA, wB, wa + o1, wa + o2, src, srcA, ldsw + o0, ldsw + o1, voff, s + 3 < R, s + 4 < R, s + 3 < R ? 8 : 0, kq == 0 && s == 2, false);
    }
    { const unsigned t = o0; o0 = o2; o2 = o1; o1 = t; }
  }
  MFMA_SETTLE();
  OST(1);
  f32x4 own[10];
  asm volatile("s_barrier" ::: "memory");
  { LAS char* mw = (LAS char*)lds + wid * 10240 + lane * 16; const LAS char* mr = lds + (wid ^ 4) * 10240 + lane * 16;
#pragma unroll
    for (int n = 0; n < 10; ++n) { *(LAS f32x4*)(mw + n * 1024) = par == 0 ? acc[1][n] : acc[0][n]; own[n] = par == 0 ? acc[0][n] : acc[1][n]; }
    asm volatile("s_waitcnt lgkmcnt(0)\n\ts_barrier" ::: "memory");
#pragma unroll
    for (int n = 0; n < 10; ++n) own[n] += *(const LAS f32x4*)(mr + n * 1024);
  }
  const rsrc_t rex = mk_rsrc(Ex, 64u * 4u * 8u * 4u * 2048u);
  { const unsigned sb = (unsigned)(((tile * 4 + kq) * 8 + wid) * 4) * 2048u;
#define SEND(o) do { if (kq != (o)) { const u32x2 p0 = pack4(own[o]), p1 = pack4(own[(o) + 4]); st128<16>((u32x4){p0[0], p0[1], p1[0], p1[1]}, rex, sb + (o) * 2048u + lane * 16u); \
      if ((o) < 2) st64<16>(pack4(own[((o) + 8) % 10]), rex, sb + (o) * 2048u + 1024u + lane * 8u); } } while (0)
    SEND(0); SEND(1); SEND(2); SEND(3);
#undef SEND
  }
  asm volatile("s_waitcnt vmcnt(0)\n\ts_barrier" ::: "memory");
  OST(2);
  if (tid == 0) __hip_atomic_store(flags + (tile * 4 + kq) * 16, 1u, RLX_AGENT);
  if (kq == 0) {
#pragma unroll
    for (int n = 0; n < 10; ++n) st64<0>(bres[n], rout, vout + 1280u + n * 32u);
  }
  if (wid == 0) {
    const unsigned long long t0 = __builtin_amdgcn_s_memrealtime();
    for (;;) {
      const unsigned v = lane < 4 ? __hip_atomic_load(flags + (tile * 4 + lane) * 16, RLX_AGENT) : 1u;
      if (__all(v == 1u)) break;
      if (__builtin_amdgcn_s_memrealtime() - t0 > 200000ull) break;
      __builtin_amdgcn_s_sleep(1);
    }
  }
  asm volatile("s_barrier" ::: "memory");
  OST(3);
  u32x4 rp[3]; u32x2 rs[3];
#pragma unroll
  for (int i = 0; i < 3; ++i) { const int src_ = (kq + 1 + i) & 3; const unsigned sb = (unsigned)(((tile * 4 + src_) * 8 + wid) * 4 + kq) * 2048u;
    rp[i] = __builtin_amdgcn_raw_buffer_load_b128(rex, sb + lane * 16u, 0, 16);
    rs[i] = __builtin_amdgcn_raw_buffer_load_b64(rex, kq < 2 ? sb + 1024u + lane * 8u : 0x80000000u, 0, 16); }
  f32x4 f0 = kq == 0 ? own[0] : kq == 1 ? own[1] : kq == 2 ? own[2] : own[3], f1 = kq == 0 ? own[4] : kq == 1 ? own[5] : kq == 2 ? own[6] : own[7], f2 = kq == 0 ? own[8] : own[9];
#pragma unroll
  for (int i = 0; i < 3; ++i) { f0 += unpack4(rp[i][0], rp[i][1]); f1 += unpack4(rp[i][2], rp[i][3]); f2 += unpack4(rs[i][0], rs[i][1]); }
  st64<0>(pack4(f0), rout, vout + (unsigned)kq * 32u); st64<0>(pack4(f1), rout, vout + (unsigned)(kq + 4) * 32u); st64<0>(pack4(f2), rout, kq < 2 ? vout + (unsigned)(kq + 8) * 32u : 0x80000000u);
}
}

extern "C" void kernel_launch(void* const* d_in, const int* in_sizes, int n_in, void* d_out, int out_size, void* d_ws, size_t ws_size, hipStream_t stream) {
  const u16* Ax = (const u16*)d_in[0]; const u16* ckv = (const u16*)d_in[1]; const u16* wq = (const u16*)d_in[2]; const u16* wuk = (const u16*)d_in[3];
  const u16* wuv = (const u16*)d_in[4]; const u16* wo = (const u16*)d_in[5]; const u16* Bx = (const u16*)d_in[6]; const u16* wqkv = (const u16*)d_in[7];
  const u16* wphi = (const u16*)d_in[8]; const u16* Bwo = (const u16*)d_in[9]; u16* out = (u16*)d_out;
  char* ws = (char*)d_ws;
  u16* Qf = (u16*)(ws); u16* Kf = (u16*)(ws + (16u << 20)); u16* Vf = (u16*)(ws + (32u << 20)); u16* Cf = (u16*)(ws + (48u << 20));
  u16* PhiQ = (u16*)(ws + (64u << 20)); u16* Of = (u16*)(ws + (72u << 20)); float* Part = (float*)(ws + (76u << 20));
  u16* WfA = (u16*)(ws + (80u << 20)); u16* WfB = (u16*)(ws + (86u << 20)); unsigned* flags = (unsigned*)(ws + (88u << 20)); u16* Ex = (u16*)(ws + (90u << 20));
  (void)hipFuncSetAttribute((const void*)proj::proj_kernel, hipFuncAttributeMaxDynamicSharedMemorySize, proj::LDS_BYTES);
  (void)hipFuncSetAttribute((const void*)attn::attn_kernel, hipFuncAttributeMaxDynamicSharedMemorySize, attn::LDS_BYTES);
  (void)hipFuncSetAttribute((const void*)outp::outp_kernel, hipFuncAttributeMaxDynamicSharedMemorySize, outp::LDS_BYTES);
  proj::proj_kernel<<<256, 512, proj::LDS_BYTES, stream>>>(Ax, ckv, wq, wuk, wuv, Bx, wqkv, Qf, Kf, Vf, wphi, PhiQ, Part, wo, Bwo, WfA, WfB, flags, nullptr);
  attn::attn_kernel<<<256, 512, attn::LDS_BYTES, stream>>>(Qf, Kf, Vf, Cf, PhiQ, Part, Of, nullptr);
  outp::outp_kernel<<<256, 512, outp::LDS_BYTES, stream>>>(Cf, Of, WfA, WfB, out, Ex, flags, nullptr);
}
```

```cpp
#include <hip/hip_runtime.h>
#include <stdint.h>
typedef unsigned short u16;
typedef short bf16x8 __attribute__((ext_vector_type(8)));
typedef short s16x4 __attribute__((ext_vector_type(4)));
typedef float f32x4 __attribute__((ext_vector_type(4)));
typedef float f32x2 __attribute__((ext_vector_type(2)));
typedef __bf16 bf16x2_t __attribute__((ext_vector_type(2)));
typedef unsigned u32x4 __attribute__((ext_vector_type(4)));
typedef unsigned u32x2 __attribute__((ext_vector_type(2)));
typedef __amdgpu_buffer_rsrc_t rsrc_t;
#define LAS __attribute__((address_space(3)))
#define DI __device__ __forceinline__
#define SBAR() __builtin_amdgcn_sched_barrier(0)
#define PIN(x) asm volatile("" : "+v"(x))
#define MFMA16(a, b, c) __builtin_amdgcn_mfma_f32_16x16x32_bf16(a, b, c, 0, 0, 0)
DI void mfma_acc(f32x4& acc, const bf16x8& a, const bf16x8& b) { asm volatile("v_mfma_f32_16x16x32_bf16 %0, %1, %2, %0" : "+v"(acc) : "v"(a), "v"(b)); }
DI void mfma_init(f32x4& d, const bf16x8& a, const bf16x8& b, const f32x4& c) { asm volatile("v_mfma_f32_16x16x32_bf16 %0, %1, %2, %3" : "=&v"(d) : "v"(a), "v"(b), "v"(c)); }
DI void mfma_zero(f32x4& d, const bf16x8& a, const bf16x8& b) { asm volatile("v_mfma_f32_16x16x32_bf16 %0, %1, %2, 0" : "=&v"(d) : "v"(a), "v"(b)); }
#define MFMA_SETTLE() do { SBAR(); asm volatile("s_nop 7\n\ts_nop 1" ::: "memory"); SBAR(); } while (0)
DI unsigned rfl(unsigned x) { return __builtin_amdgcn_readfirstlane(x); }
DI unsigned cvtpk(float lo, float hi) { f32x2 v = {lo, hi}; bf16x2_t b = __builtin_convertvector(v, bf16x2_t); return __builtin_bit_cast(unsigned, b); }
DI float bf_lo(unsigned w) { return __uint_as_float(w << 16); }
DI float bf_hi(unsigned w) { return __uint_as_float(w & 0xffff0000u); }
DI rsrc_t mk_rsrc(const void* p, unsigned bytes) { return __builtin_amdgcn_make_buffer_rsrc((void*)p, (short)0, (int)bytes, 0x00020000); }
DI bf16x8 lds_rd128(const LAS char* p) { return *(const LAS bf16x8*)p; }
typedef short v4i16_t __attribute__((ext_vector_type(4)));
DI s16x4 lds_rdtr(const LAS char* p) { return __builtin_bit_cast(s16x4, __builtin_amdgcn_ds_read_tr16_b64_v4i16((LAS v4i16_t*)p)); }
DI float max3f(float a, float b, float c) { float r; asm("v_max3_f32 %0, %1, %2, %3" : "=v"(r) : "v"(a), "v"(b), "v"(c)); return r; }
DI float max3abs(float a, float b, float c) { float r; asm("v_max3_f32 %0, %1, |%2|, |%3|" : "=v"(r) : "v"(a), "v"(b), "v"(c)); return r; }
DI float fadd1(float a, float b) { float r; asm("v_add_f32_e32 %0, %1, %2" : "=v"(r) : "v"(a), "v"(b)); return r; }
DI void set_m0(unsigned a) { asm volatile("s_mov_b32 m0, %0\n\ts_nop 0" ::"s"(a) : "memory"); }
template <int OFF> DI void dma16m(unsigned voff, rsrc_t srd, unsigned soff) {
  asm volatile("buffer_load_dwordx4 %0, %1, %2 offen offset:%c3 lds" ::"v"(voff), "s"(srd), "s"(soff), "i"(OFF) : "memory");
}
DI void dma16(unsigned ldsaddr, unsigned voff, rsrc_t srd, unsigned soff) {
  asm volatile("s_mov_b32 m0, %0\n\ts_nop 0\n\tbuffer_load_dwordx4 %1, %2, %3 offen lds" ::"s"(ldsaddr), "v"(voff), "s"(srd), "s"(soff) : "memory");
}
typedef unsigned srd_t __attribute__((ext_vector_type(4)));
DI srd_t mk_srd(const void* p, unsigned bytes) { const unsigned long long a = (unsigned long long)p; return (srd_t){(unsigned)a, (unsigned)(a >> 32) & 0xffffu, bytes, 0x00020000u}; }
DI void dma16s(unsigned ldsaddr, unsigned voff, srd_t srd, unsigned soff) {
  asm volatile("s_mov_b32 m0, %0\n\ts_nop 0\n\tbuffer_load_dwordx4 %1, %2, %3 offen lds" ::"s"(ldsaddr), "v"(voff), "s"(srd), "s"(soff) : "memory");
}
DI void touch_lds(unsigned ldsaddr, unsigned voff, rsrc_t srd, unsigned soff) {
  asm volatile("s_mov_b32 m0, %0\n\ts_nop 0\n\tbuffer_load_dword %1, %2, %3 offen lds" ::"s"(ldsaddr), "v"(voff), "s"(srd), "s"(soff) : "memory");
}
#define BULK_BITS "sc0 sc1"
DI void touch_lds_far(unsigned ldsaddr, unsigned voff, rsrc_t srd, unsigned soff) {
  asm volatile("s_mov_b32 m0, %0\n\ts_nop 0\n\tbuffer_load_dword %1, %2, %3 offen " BULK_BITS " lds" ::"s"(ldsaddr), "v"(voff), "s"(srd), "s"(soff) : "memory");
}
template <int AUX> DI void st128(const u32x4& w, rsrc_t r, unsigned voff) { asm volatile("" : "+v"(voff)); __builtin_amdgcn_raw_buffer_store_b128(w, r, voff, 0, AUX); }
template <int AUX> DI void st64(const u32x2& w, rsrc_t r, unsigned voff) { asm volatile("" : "+v"(voff)); __builtin_amdgcn_raw_buffer_store_b64(w, r, voff, 0, AUX); }
DI float xrow_max(float x) {
  auto a = __builtin_amdgcn_permlane32_swap(__float_as_uint(x), __float_as_uint(x), false, false); x = fmaxf(__uint_as_float(a[0]), __uint_as_float(a[1]));
  auto b = __builtin_amdgcn_permlane16_swap(__float_as_uint(x), __float_as_uint(x), false, false); return fmaxf(__uint_as_float(b[0]), __uint_as_float(b[1]));
}
DI float xrow_sum(float x) {
  auto a = __builtin_amdgcn_permlane32_swap(__float_as_uint(x), __float_as_uint(x), false, false); x = __uint_as_float(a[0]) + __uint_as_float(a[1]);
  auto b = __builtin_amdgcn_permlane16_swap(__float_as_uint(x), __float_as_uint(x), false, false); return __uint_as_float(b[0]) + __uint_as_float(b[1]);
}
template <bool MAXOP> DI float xop(float a, float b) { if (MAXOP) return __builtin_amdgcn_fmed3f(a, b, __builtin_inff()); else return a + b; }
template <bool MAXOP> DI void xrow4(float& a, float& b, float& c, float& d) {
  auto x = __builtin_amdgcn_permlane32_swap(__float_as_uint(a), __float_as_uint(b), false, false); const float u = xop<MAXOP>(__uint_as_float(x[0]), __uint_as_float(x[1]));
  auto y = __builtin_amdgcn_permlane32_swap(__float_as_uint(c), __float_as_uint(d), false, false); const float v = xop<MAXOP>(__uint_as_float(y[0]), __uint_as_float(y[1]));
  auto z = __builtin_amdgcn_permlane16_swap(__float_as_uint(u), __float_as_uint(v), false, false); const float w = xop<MAXOP>(__uint_as_float(z[0]), __uint_as_float(z[1]));
  auto s = __builtin_amdgcn_permlane32_swap(__float_as_uint(w), __float_as_uint(w), false, false);
  auto p = __builtin_amdgcn_permlane16_swap(s[0], s[0], false, false); auto q = __builtin_amdgcn_permlane16_swap(s[1], s[1], false, false);
  a = __uint_as_float(p[0]); c = __uint_as_float(p[1]); b = __uint_as_float(q[0]); d = __uint_as_float(q[1]);
}
DI u32x4 pair_swap(unsigned x0, unsigned x1, unsigned y0, unsigned y1) {
  auto a = __builtin_amdgcn_permlane16_swap(x0, y0, false, false);
  auto b = __builtin_amdgcn_permlane16_swap(x1, y1, false, false);
  return (u32x4){a[0], b[0], a[1], b[1]};
}

#define NOWARM 1
namespace proj {
constexpr int LDS_X = 0, LDS_W = 65536, LDS_T = 131072, LDS_BYTES = 163840;
constexpr float QSCALE = 0.08838834764831845f * 1.4426950408889634f;

struct Src {
  rsrc_t X, W; unsigned vX[4], vW[2]; unsigned sX, sW, wstep, rowX8;
  unsigned rowW2;
};
struct Frag { bf16x8 x[8]; s16x4 wl[2][4], wh[2][4]; };
#define WF(ks, n) __builtin_shufflevector(fr.wl[ks][n], fr.wh[ks][n], 0, 1, 2, 3, 4, 5, 6, 7)
struct Adr { const LAS char* x[2][2]; const LAS char* w[4]; };

enum { U_Q = 0, U_K = 1, U_V = 2, U_B = 3 };
struct Unit { int kind, pm, pn; };

struct Ptrs { const u16 *Ax, *ckv, *Bx, *wq, *wuk, *wuv, *wqkv; };
DI int fresh_lane() { int l; asm volatile("v_mbcnt_lo_u32_b32 %0, -1, 0\n\tv_mbcnt_hi_u32_b32 %0, -1, %0" : "=v"(l)); return l; }
DI void setup_src(Src& s, const Unit& u, const Ptrs& P, int wid) {
  const int lane = fresh_lane();
  const u16 *Ax = P.Ax, *ckv = P.ckv, *Bx = P.Bx, *wq = P.wq, *wuk = P.wuk, *wuv = P.wuv, *wqkv = P.wqkv;
  const int ldx = (u.kind == U_K || u.kind == U_V) ? 512 : 640;
  const u16* X = u.kind == U_Q ? Ax : (u.kind == U_B ? Bx : ckv);
  s.X = mk_rsrc(X, 2048u * (unsigned)ldx * 2u);
  const int r = lane >> 3, f0 = ((r >> 1) & 1) | (((r >> 2) & 1) << 1);
#pragma unroll
  for (int i = 0; i < 4; ++i) s.vX[i] = (unsigned)(r * ldx * 2) + (unsigned)((((lane & 7) ^ f0 ^ (((i >> 1) & 1) << 1) ^ ((i & 1) << 2)) & 7) * 16);
  s.rowX8 = (unsigned)(8 * ldx * 2);
  s.sX = (unsigned)((u.pm * 256 + wid * 32) * ldx * 2);
  if (u.kind == U_B) {
    s.W = mk_rsrc(wqkv, 640u * 1920u * 2u);
#pragma unroll
    for (int p = 0; p < 2; ++p) { const int sg = ((2 * p + (lane >> 5)) & 3) | ((wid & 1) << 2); const int ch = (lane & 31) ^ (sg << 1);
      const int col = ch < 10 ? u.pn * 160 + ch * 16 : (ch < 20 ? 1280 + u.pn * 160 + (ch - 10) * 16 : (ch < 30 ? 2560 + u.pn * 160 + (ch - 20) * 16 : u.pn * 160));
      s.vW[p] = (unsigned)((lane >> 5) * 3840 + col); }
    s.rowW2 = 2u * 3840u; s.wstep = 64u * 3840u; s.sW = (unsigned)(wid * 8 * 3840);
  } else {
    const u16* W = u.kind == U_Q ? wq : (u.kind == U_K ? wuk : wuv);
    s.W = mk_rsrc(W, (u.kind == U_Q ? 640u : 512u) * 4096u * 2u);
#pragma unroll
    for (int p = 0; p < 2; ++p) { const int sg = ((2 * p + (lane >> 5)) & 3) | ((wid & 1) << 2); s.vW[p] = (unsigned)((lane >> 5) * 8192 + (((lane & 31) ^ (sg << 1)) * 16)); }
    s.rowW2 = 2u * 8192u; s.wstep = 64u * 8192u; s.sW = (unsigned)(wid * 8 * 8192 + u.pn * 512);
  }
}
template <int J> DI void dma_piece(const Src& s, unsigned ldsw, int slot) {
  if constexpr (J < 4) dma16(ldsw + LDS_X + slot * 32768 + J * 1024, s.vX[J], s.X, s.sX + J * s.rowX8);
  else dma16(ldsw + LDS_W + slot * 32768 + (J - 4) * 1024, s.vW[(J - 4) & 1], s.W, s.sW + (J - 4) * s.rowW2);
}
DI void src_next(Src& s) { s.sX += 128u; s.sW += s.wstep; }
struct Warm { rsrc_t r; unsigned voff, soff, step, ldsa; };
DI void setup_warm(Warm& w, const Unit& u, bool on, int tile0, const Ptrs& P, int wid, unsigned ldsa) {
  const int lane = fresh_lane();
  const bool kv = u.kind == U_K || u.kind == U_V;
  const int ldx = kv ? 512 : 640;
  const u16* X = u.kind == U_Q ? P.Ax : (u.kind == U_B ? P.Bx : P.ckv);
  const u16* W = u.kind == U_Q ? P.wq : (u.kind == U_K ? P.wuk : (u.kind == U_V ? P.wuv : P.wqkv));
  const unsigned wbytes = u.kind == U_B ? 640u * 1920u * 2u : (u.kind == U_Q ? 640u : 512u) * 4096u * 2u;
  w.r = mk_rsrc(wid < 4 ? X : W, wid < 4 ? 2048u * (unsigned)ldx * 2u : wbytes);
  if (wid < 4) { const int r = 64 * wid + 2 * (lane & 31) + (u.pn & 1);
    w.voff = (on && lane < 32) ? (unsigned)(r * ldx * 2) : 0x80000000u; w.step = 128u; w.soff = (unsigned)(u.pm * 256 * ldx * 2 + tile0 * 128); }
  else { const int kk = 16 * (wid - 4) + (lane & 15), sh = u.pm & 3;
    if (u.kind == U_B) { const int seg = sh < 3 ? sh : 0; w.voff = (on && lane < 32) ? (unsigned)(kk * 3840 + seg * 1280 + u.pn * 160 + (lane >> 4) * 156) : 0x80000000u; w.step = 64u * 3840u; w.soff = (unsigned)tile0 * w.step; }
    else { w.voff = (on && lane < 16) ? (unsigned)(kk * 8192 + u.pn * 512 + sh * 128) : 0x80000000u; w.step = 64u * 8192u; w.soff = (unsigned)tile0 * w.step; } }
  w.soff = rfl(w.soff); w.step = rfl(w.step); w.ldsa = ldsa;
}
DI void warm(Warm& w) { }

DI void setup_adr(Adr& a, const LAS char* lds, bool kappa, int wr, int wc) {
  const int lane = fresh_lane();
  const int c = lane & 15, g = lane >> 4;
#pragma unroll
  for (int par = 0; par < 2; ++par) {
    const int row = kappa ? 8 * (c >> 2) + 4 * par + (c & 3) : 16 * par + c;
    const int f = ((row >> 1) & 1) | ((((row >> 2) ^ (row >> 4)) & 1) << 1) | (((row >> 3) & 1) << 2);
#pragma unroll
    for (int ks = 0; ks < 2; ++ks) a.x[par][ks] = lds + LDS_X + wr * 16384 + row * 128 + (((4 * ks + g) ^ f) * 16);
  }
  const int q = (lane & 15) >> 2, p = lane & 3, sg = q | ((g & 1) << 2);
#pragma unroll
  for (int n = 0; n < 4; ++n) a.w[n] = lds + LDS_W + (8 * g + q) * 512 + (((4 * wc + n) ^ sg) * 32) + 8 * p;
}
#define BULK_STRIDE 128
DI void bulk_touch(int chunk, const Ptrs& P, const u16* wo, const u16* Bwo, unsigned ldsa, int lane) {
  constexpr unsigned CB = 64u * BULK_STRIDE;
  constexpr unsigned n0 = 2621440u / CB, n1 = n0 + 5242880u / CB, n2 = n1 + 2621440u / CB, n3 = n2 + 2457600u / CB, n4 = n3 + 2097152u / CB, n5 = n4 + 4194304u / CB, n6 = n5 + 4194304u / CB, n7 = n6 + 5242880u / CB, n8 = n7 + 819200u / CB;
  const unsigned c = (unsigned)chunk;
  const u16* p = c < n0 ? P.Ax : c < n1 ? P.wq : c < n2 ? P.Bx : c < n3 ? P.wqkv : c < n4 ? P.ckv : c < n5 ? P.wuk : c < n6 ? P.wuv : c < n7 ? wo : Bwo;
  const unsigned b = c < n0 ? 0u : c < n1 ? n0 : c < n2 ? n1 : c < n3 ? n2 : c < n4 ? n3 : c < n5 ? n4 : c < n6 ? n5 : c < n7 ? n6 : n7;
  touch_lds_far(ldsa, c < n8 ? (unsigned)lane * BULK_STRIDE : 0x80000000u, mk_rsrc(p, 0x7fffffffu), rfl((c - b) * CB));
}
constexpr int BULK_N = (BULK_STRIDE == 128) ? 2 : 4;
#define RDX(SL, f) (fr.x[(f) & 7] = lds_rd128(ad.x[(f) & 1][((f) >> 3) & 1] + (SL) * 32768 + (((f) & 7) >> 1) * 4096))
#define RDW(SL, ks, n, h) do { if (h) fr.wh[ks][n] = lds_rdtr(ad.w[n] + (SL) * 32768 + (ks) * 16384 + 2048); else fr.wl[ks][n] = lds_rdtr(ad.w[n] + (SL) * 32768 + (ks) * 16384); } while (0)

#define ST_AUX 16
struct Epi { rsrc_t r; unsigned voff; float scale; bool on; };
DI void setup_epi(Epi& e, const Unit& u, u16* Qf, u16* Kf, u16* Vf, int wr, int wc) {
  const int lane = fresh_lane(), g = lane >> 4;
  e.on = u.kind != U_B; e.scale = u.kind == U_Q ? QSCALE : 1.f;
  e.r = mk_rsrc(u.kind == U_Q ? Qf : (u.kind == U_K ? Kf : Vf), 2048u * 4096u * 2u);
  if (u.kind == U_V) e.voff = (unsigned)(((u.pn * 2 + (wc >> 1)) * 64 + u.pm * 8 + wr * 4) * 8 + (wc & 1) * 4) * 1024u + (unsigned)lane * 16u;
  else e.voff = (unsigned)(((u.pn * 2 + (wc >> 1)) * 128 + u.pm * 16 + wr * 8) * 4 + (wc & 1) * 2) * 1024u + (unsigned)((lane & 15) + 16 * (((g & 1) << 1) | (g >> 1))) * 16u;
}
template <int M> DI void epi_qk(const f32x4 (&acc)[8][4], const Epi& e) {
#pragma unroll
  for (int t = 0; t < 2; ++t) { const f32x4 a = acc[M][2 * t] * e.scale, b = acc[M][2 * t + 1] * e.scale;
    st128<ST_AUX>(pair_swap(cvtpk(a[0], a[1]), cvtpk(a[2], a[3]), cvtpk(b[0], b[1]), cvtpk(b[2], b[3])), e.r, e.voff + M * 4096 + t * 1024); }
}
template <int MM, int H> DI void epi_v(const f32x4 (&acc)[8][4], const Epi& e) {
#pragma unroll
  for (int n = 2 * H; n < 2 * H + 2; ++n) { const f32x4 a = acc[2 * MM][n], b = acc[2 * MM + 1][n];
    st128<ST_AUX>((u32x4){cvtpk(a[0], a[1]), cvtpk(a[2], a[3]), cvtpk(b[0], b[1]), cvtpk(b[2], b[3])}, e.r, e.voff + MM * 8192 + n * 1024); }
}

#define XWAIT(FIRST) do { if (FIRST) asm volatile("s_waitcnt vmcnt(%0) lgkmcnt(0)\n\ts_barrier" ::"i"(BULK_N) : "memory"); else asm volatile("s_waitcnt vmcnt(0) lgkmcnt(0)\n\ts_barrier" ::: "memory"); } while (0)
template <int SL, bool FIRST, bool SWAP, bool LAST = false>
DI void ktile(f32x4 (&acc)[8][4], Frag& fr, const Adr& ad, Src& src, Warm& wm, const Unit& nu, const Ptrs& P, int wid, bool switch_src, bool do_dma, bool switch_warm, bool has_next, unsigned ldsw, const Epi& ep) {
#define MM(f, n) do { \
    if (FIRST && (f) < 8) { if (SWAP) mfma_zero(acc[(f) & 7][n], fr.x[(f) & 7], WF(0, n)); else mfma_zero(acc[(f) & 7][n], WF(0, n), fr.x[(f) & 7]); } \
    else { if (SWAP) mfma_acc(acc[(f) & 7][n], fr.x[(f) & 7], WF((f) >> 3, n)); else mfma_acc(acc[(f) & 7][n], WF((f) >> 3, n), fr.x[(f) & 7]); } SBAR(); } while (0)
#define GRP(f) do { \
    if ((f) == 9) { XWAIT(FIRST); SBAR(); if (switch_src) setup_src(src, nu, P, wid); if (switch_warm) setup_warm(wm, nu, has_next, 0, P, wid, wm.ldsa); } \
    MM(f, 0); \
    if ((f) < 4) { RDW(SL, 1, (f) & 3, 0); } else if (!LAST && (f) >= 9 && (f) < 13) { RDW(SL ^ 1, 0, ((f) - 9) & 3, 0); } \
    SBAR(); \
    MM(f, 1); \
    if ((f) < 4) { RDW(SL, 1, (f) & 3, 1); } else if (!LAST && (f) >= 9 && (f) < 13) { RDW(SL ^ 1, 0, ((f) - 9) & 3, 1); } \
    SBAR(); \
    MM(f, 2); \
    if (do_dma) { if ((f) == 9) dma_piece<0>(src, ldsw, SL); if ((f) == 10) dma_piece<4>(src, ldsw, SL); if ((f) == 11) dma_piece<1>(src, ldsw, SL); if ((f) == 12) dma_piece<5>(src, ldsw, SL); \
                  if ((f) == 13) dma_piece<2>(src, ldsw, SL); if ((f) == 14) dma_piece<6>(src, ldsw, SL); if ((f) == 15) { dma_piece<3>(src, ldsw, SL); } } \
    SBAR(); \
    MM(f, 3); \
    if ((f) == 15) { if (do_dma) { dma_piece<7>(src, ldsw, SL); src_next(src); } warm(wm); } \
    if ((f) < 8) RDX(SL, (f) + 8); else if (!LAST) { if ((f) == 9) { RDX(SL ^ 1, 0); SBAR(); RDX(SL ^ 1, 1); } else if ((f) > 9) RDX(SL ^ 1, (f) - 8); } \
    SBAR(); \
    if (LAST && (f) >= 10 && ep.on) {   \
      if (!SWAP) epi_qk<((f) >= 10 ? (f) - 10 : 0)>(acc, ep); else if ((f) >= 11) epi_v<((f) >= 11 ? ((f) - 11) >> 1 : 0), ((f) - 11) & 1>(acc, ep); SBAR(); } \
    } while (0)
  GRP(0); GRP(1); GRP(2); GRP(3); GRP(4); GRP(5); GRP(6); GRP(7); GRP(8); GRP(9); GRP(10); GRP(11); GRP(12); GRP(13); GRP(14); GRP(15);
#undef GRP
#undef MM
}

template <bool SWAP>
DI void run_unit(f32x4 (&acc)[8][4], const Adr& ad, Src& src, Warm& wm, const Unit& nu, const Ptrs& P, int wid, bool has_next, int nt, unsigned ldsw, const Unit& cu, u16* Qf, u16* Kf, u16* Vf) {
  Epi ep; ep.on = false; ep.scale = 1.f; ep.voff = 0; ep.r = src.X;
  Frag fr;
  if (SWAP) asm volatile("; unit: tokens on rows" ::: "memory"); else asm volatile("; unit: weights on rows" ::: "memory");
  RDW(0, 0, 0, 0); RDW(0, 0, 0, 1); RDX(0, 0); RDW(0, 0, 1, 0); RDW(0, 0, 1, 1); RDW(0, 0, 2, 0); RDW(0, 0, 2, 1); RDW(0, 0, 3, 0); RDW(0, 0, 3, 1);
  RDX(0, 1); RDX(0, 2); RDX(0, 3); RDX(0, 4); RDX(0, 5); RDX(0, 6); RDX(0, 7);
  SBAR();
  ktile<0, true, SWAP>(acc, fr, ad, src, wm, nu, P, wid, false, true, false, has_next, ldsw, ep);
  ktile<1, false, SWAP>(acc, fr, ad, src, wm, nu, P, wid, false, true, false, has_next, ldsw, ep);
  for (int t = 2; t < nt - 2; t += 2) {
    ktile<0, false, SWAP>(acc, fr, ad, src, wm, nu, P, wid, false, true, t == nt - 4, has_next, ldsw, ep);
    ktile<1, false, SWAP>(acc, fr, ad, src, wm, nu, P, wid, false, true, false, has_next, ldsw, ep);
  }
  ktile<0, false, SWAP>(acc, fr, ad, src, wm, nu, P, wid, has_next, has_next, false, has_next, ldsw, ep);
  setup_epi(ep, cu, Qf, Kf, Vf, wid >> 2, wid & 3);
  ktile<1, false, SWAP, true>(acc, fr, ad, src, wm, nu, P, wid, false, has_next, false, has_next, ldsw, ep);
  MFMA_SETTLE();
  if (ep.on) {
    if (!SWAP) { epi_qk<6>(acc, ep); epi_qk<7>(acc, ep); } else { epi_v<2, 1>(acc, ep); epi_v<3, 0>(acc, ep); epi_v<3, 1>(acc, ep); }
  }
}

constexpr int T_PITCH = 528, PH_OFF = 256 * T_PITCH, PH_PITCH = 96, PH_WAVE = 32 * PH_PITCH;
static_assert(PH_OFF + 8 * PH_WAVE + 6 * 1024 <= LDS_BYTES + 6 * 1024 && PH_OFF + 8 * PH_WAVE <= LDS_BYTES, "LDS map");
#define BSTAMP(i)
DI float min3f(float a, float b, float c) { float r; asm("v_min3_f32 %0, %1, %2, %3" : "=v"(r) : "v"(a), "v"(b), "v"(c)); return r; }
constexpr int WPHI_OFF = 159744;
DI void epi_b(const f32x4 (&acc)[8][4], const Unit& u, const LAS char* lds_c, u16* __restrict__ PhiQ, float* __restrict__ Part, int wid, unsigned long long* bts) {
  LAS char* lds = (LAS char*)lds_c;
  const int lane = fresh_lane(), c = lane & 15, g = lane >> 4, gp = ((g & 1) << 1) | (g >> 1), wr = wid >> 2, wc = wid & 3;
  const int hh = wid & 3, half = wid >> 2, head = u.pn * 4 + hh;
  asm volatile("s_waitcnt vmcnt(0) lgkmcnt(0)\n\ts_barrier" ::: "memory");
#pragma unroll
  for (int m = 0; m < 8; ++m)
#pragma unroll
    for (int t = 0; t < 2; ++t) { const f32x4 a = acc[m][2 * t], b = acc[m][2 * t + 1];
      *(LAS u32x4*)(lds + (wr * 128 + m * 16 + c) * T_PITCH + (wc * 64 + t * 32 + gp * 8) * 2) = pair_swap(cvtpk(a[0], a[1]), cvtpk(a[2], a[3]), cvtpk(b[0], b[1]), cvtpk(b[2], b[3])); }
  SBAR();
  bf16x8 w0, w1;
  { const LAS u16* wp = (const LAS u16*)(lds + WPHI_OFF) + hh * 400;
#pragma unroll
    for (int j = 0; j < 8; ++j) { const int d = 8 * g + j, dd = d < 20 ? d : 0; const u16 a = wp[dd * 20 + c], b = wp[dd * 20 + 16 + (c & 3)];
      w0[j] = d < 20 ? (short)a : (short)0; w1[j] = (d < 20 && c < 8) ? (short)(c < 4 ? b : (u16)(b ^ 0x8000u)) : (short)0; } }
  asm volatile("s_waitcnt lgkmcnt(0)\n\ts_barrier" ::: "memory");
  BSTAMP(0);
  LAS char* ph = lds + PH_OFF + wid * PH_WAVE;
  const rsrc_t rq = mk_rsrc(PhiQ, 32u * 128u * 1280u);
  const unsigned vq0 = (unsigned)lane * 16u, vq1 = g < 2 ? 1024u + (unsigned)c * 16u + (unsigned)g * 8u : 0x80000000u;
  f32x4 kv[3][2];
#pragma unroll
  for (int i = 0; i < 3; ++i) { kv[i][0] = (f32x4){}; kv[i][1] = (f32x4){}; }
  const int q4 = c >> 2, p4 = c & 3;
  const unsigned mlo = g == 3 ? 0u : 0xffffffffu, mhi = g >= 2 ? 0u : 0xffffffffu;
  const float NEG = -__builtin_inff(), POS = __builtin_inff();
  constexpr float L2E = 1.4426950408889634f;
  for (int step = 0; step < 4; ++step) {
    const int r0 = half * 128 + step * 32;
    f32x4 a[4], b[4];
#pragma unroll
    for (int i = 0; i < 4; ++i) { const LAS char* xr = lds + (r0 + (i >> 1) * 16 + c) * T_PITCH + ((i & 1) * 80 + hh * 20 + 8 * g) * 2;
      u32x2 lo = *(const LAS u32x2*)xr, hi = *(const LAS u32x2*)(xr + 8); lo &= mlo; hi &= mhi;
      const bf16x8 x = __builtin_bit_cast(bf16x8, (u32x4){lo[0], lo[1], hi[0], hi[1]});
      a[i] = MFMA16(w0, x, ((f32x4){})); b[i] = MFMA16(w1, x, ((f32x4){})); }
    asm volatile("s_nop 7\n\ts_nop 1" : "+v"(a[0]), "+v"(a[1]), "+v"(a[2]), "+v"(a[3]), "+v"(b[0]), "+v"(b[1]), "+v"(b[2]), "+v"(b[3]));
    float mx[4], mn[4];
#pragma unroll
    for (int i = 0; i < 4; ++i) {
      const float bm = max3f(max3f(b[i][0], b[i][1], b[i][2]), b[i][3], NEG);
      mx[i] = max3f(max3f(a[i][0], a[i][1], a[i][2]), a[i][3], g == 0 ? bm : NEG); mn[i] = max3f(-min3f(min3f(a[i][0], a[i][1], a[i][2]), a[i][3], POS), g == 1 ? bm : NEG, NEG); }
    asm volatile("s_nop 1" : "+v"(mx[0]), "+v"(mx[1]), "+v"(mx[2]), "+v"(mx[3]), "+v"(mn[0]), "+v"(mn[1]), "+v"(mn[2]), "+v"(mn[3]));
    xrow4<true>(mx[0], mx[1], mx[2], mx[3]); xrow4<true>(mn[0], mn[1], mn[2], mn[3]);
#pragma unroll
    for (int i = 0; i < 4; ++i) { mx[i] *= L2E; mn[i] *= L2E; }
    float sp[4], sm[4]; f32x4 M0[4];
#pragma unroll
    for (int i = 0; i < 4; ++i) { const float rb = g == 0 ? mx[i] : (g == 1 ? mn[i] : POS);
      f32x4 p0, m0;
#pragma unroll
      for (int r = 0; r < 4; ++r) { p0[r] = __builtin_amdgcn_exp2f(fmaf(a[i][r], L2E, -mx[i])); m0[r] = __builtin_amdgcn_exp2f(fmaf(a[i][r], -L2E, -mn[i])); b[i][r] = __builtin_amdgcn_exp2f(fmaf(b[i][r], L2E, -rb)); }
      const float sb = (b[i][0] + b[i][1]) + (b[i][2] + b[i][3]);
      sp[i] = ((p0[0] + p0[1]) + (p0[2] + p0[3])) + (g == 0 ? sb : 0.f); sm[i] = ((m0[0] + m0[1]) + (m0[2] + m0[3])) + (g == 1 ? sb : 0.f);
      a[i] = p0; M0[i] = m0; }
    xrow4<false>(sp[0], sp[1], sp[2], sp[3]); xrow4<false>(sm[0], sm[1], sm[2], sm[3]);
#pragma unroll
    for (int i = 0; i < 4; ++i) { sp[i] = __builtin_amdgcn_rcpf(sp[i]); sm[i] = __builtin_amdgcn_rcpf(sm[i]); }
#pragma unroll
    for (int i = 0; i < 4; ++i) {
      const f32x4 p = a[i] * sp[i], m = M0[i] * sm[i], e = b[i] * (g == 0 ? sp[i] : sm[i]);
      const u32x4 f0 = {cvtpk(p[0], p[1]), cvtpk(p[2], p[3]), cvtpk(m[0], m[1]), cvtpk(m[2], m[3])};
      const u32x2 f1 = {cvtpk(e[0], e[1]), cvtpk(e[2], e[3])};
      if ((i & 1) == 0) {
        const unsigned so = (unsigned)(head * 128 + u.pm * 16 + (r0 >> 4) + (i >> 1)) * 1280u;
        st128<ST_AUX>(f0, rq, vq0 + so); st64<ST_AUX>(f1, rq, vq1 + so);
      } else {
        LAS char* pr = ph + ((i >> 1) * 16 + c) * PH_PITCH + 8 * g;
        *(LAS u32x2*)(pr) = (u32x2){f0[0], f0[1]}; *(LAS u32x2*)(pr + 32) = (u32x2){f0[2], f0[3]}; *(LAS u32x2*)(pr + 64) = f1;
      }
    }
    asm volatile("s_waitcnt lgkmcnt(0)" ::: "memory");
    bf16x8 vb[2];
#pragma unroll
    for (int db = 0; db < 2; ++db) { const LAS char* av = lds + (r0 + 8 * g + q4) * T_PITCH + (160 + hh * 20 + 16 * db + 4 * p4) * 2;
      vb[db] = __builtin_shufflevector(lds_rdtr(av), lds_rdtr(av + 4 * T_PITCH), 0, 1, 2, 3, 4, 5, 6, 7); }
    if (c == 4) vb[1] = (bf16x8){0x3F80, 0x3F80, 0x3F80, 0x3F80, 0x3F80, 0x3F80, 0x3F80, 0x3F80};
#pragma unroll
    for (int ab = 0; ab < 3; ++ab) { const LAS char* ap = ph + (8 * g + q4) * PH_PITCH + (16 * ab + 4 * p4) * 2;
      const bf16x8 pa = __builtin_shufflevector(lds_rdtr(ap), lds_rdtr(ap + 4 * PH_PITCH), 0, 1, 2, 3, 4, 5, 6, 7);
      kv[ab][0] = MFMA16(pa, vb[0], kv[ab][0]); kv[ab][1] = MFMA16(pa, vb[1], kv[ab][1]); }
    asm volatile("s_waitcnt lgkmcnt(0)" ::: "memory");
  }
  BSTAMP(1);
  asm volatile("s_barrier" ::: "memory");
  LAS char* ex = lds + PH_OFF + hh * 6144 + lane * 16;
  if (half == 1) {
#pragma unroll
    for (int i = 0; i < 6; ++i) *(LAS f32x4*)(ex + i * 1024) = kv[i >> 1][i & 1]; }
  asm volatile("s_waitcnt lgkmcnt(0)\n\ts_barrier" ::: "memory");
  if (half == 0) {
#pragma unroll
    for (int i = 0; i < 6; ++i) ((f32x4*)Part)[(((size_t)u.pm * 32 + head) * 6 + i) * 64 + lane] = kv[i >> 1][i & 1] + *(const LAS f32x4*)(ex + i * 1024); }
}
DI void relayout_issue(const u16* __restrict__ wo, const u16* __restrict__ Bwo, const LAS char* lds, int wid, int gw) {
  const int lane = fresh_lane();
  const unsigned ldsw = (unsigned)(uintptr_t)lds + (unsigned)wid * 16384u;
  const unsigned voff = (unsigned)((lane >> 3) * 1280 + (lane & 7) * 16);
#pragma unroll
  for (int e = 0; e < 3; ++e) { const int x = gw + e * 512; const int ks = x / 10, ng = x - ks * 10; const bool isA = ks < 128;
    const rsrc_t r = mk_rsrc(isA ? wo : Bwo, x < 1480 ? (isA ? 4096u * 640u * 2u : 640u * 640u * 2u) : 0u);
    const unsigned so = (unsigned)((isA ? ks : ks - 128) * 32 * 1280 + ng * 128);
#pragma unroll
    for (int n = 0; n < 4; ++n) dma16(ldsw + (unsigned)(e * 4 + n) * 1024u, voff, r, so + n * 8u * 1280u); }
}
DI void relayout_finish(u16* __restrict__ WfA, u16* __restrict__ WfB, const LAS char* lds, int wid, int gw) {
  const int lane = fresh_lane(), g = lane >> 4, q4 = (lane & 15) >> 2, p4 = lane & 3;
  const LAS char* rd = lds + wid * 16384 + (8 * g + q4) * 128 + 8 * p4;
  const rsrc_t rA = mk_rsrc(WfA, 4096u * 640u * 2u), rB = mk_rsrc(WfB, 640u * 640u * 2u);
  asm volatile("s_waitcnt vmcnt(0)" ::: "memory");
#pragma unroll
  for (int e = 0; e < 3; ++e) { const int x = gw + e * 512; const int ks = x / 10, ng = x - ks * 10;
#pragma unroll
    for (int n = 0; n < 4; ++n) { const LAS char* a = rd + e * 4096 + n * 32;
      const bf16x8 w = __builtin_shufflevector(lds_rdtr(a), lds_rdtr(a + 4 * 128), 0, 1, 2, 3, 4, 5, 6, 7);
      const unsigned off = (unsigned)(((ks < 128 ? ks : ks - 128) * 40 + ng * 4 + n) * 64 + lane) * 16u;
      st128<0>(__builtin_bit_cast(u32x4, w), ks < 128 ? rA : rB, x < 1480 ? off : 0x80000000u); } }
}

__global__ __launch_bounds__(512, 2) void proj_kernel(const u16* __restrict__ Ax, const u16* __restrict__ ckv, const u16* __restrict__ wq, const u16* __restrict__ wuk, const u16* __restrict__ wuv,
                                                      const u16* __restrict__ Bx, const u16* __restrict__ wqkv, u16* __restrict__ Qf, u16* __restrict__ Kf, u16* __restrict__ Vf,
                                                      const u16* __restrict__ wphi, u16* __restrict__ PhiQ, float* __restrict__ Part,
                                                      const u16* __restrict__ wo, const u16* __restrict__ Bwo, u16* __restrict__ WfA, u16* __restrict__ WfB, unsigned* __restrict__ flags, unsigned long long* dbg) {
  extern __shared__ __attribute__((aligned(16))) char smem[];
  const LAS char* lds = (const LAS char*)smem;
  const int tid = threadIdx.x, lane = tid & 63, wid = (int)rfl(tid >> 6), wr = wid >> 2, wc = wid & 3;
  const unsigned ldsw = (unsigned)(uintptr_t)lds + (unsigned)wid * 4096u;
  const int x = blockIdx.x & 7, j = blockIdx.x >> 3;
  auto kv_unit = [&](int k) { Unit u; u.kind = (k & 2) ? U_V : U_K; u.pn = 2 * x + (k & 1); u.pm = k >> 2; return u; };
  Unit u0, u1; bool two = true;
  if (j < 16) { u0.kind = U_Q; u0.pn = 2 * x + (j & 1); u0.pm = j >> 1; u1 = kv_unit(j); }
  else if (j < 24) { u0 = kv_unit(16 + 2 * (j - 16)); u1 = kv_unit(17 + 2 * (j - 16)); }
  else { u0.kind = U_B; u0.pn = x; u0.pm = j - 24; u1 = u0; two = false; }
  const Ptrs P = {Ax, ckv, Bx, wq, wuk, wuv, wqkv};
  Src src;
  setup_src(src, u0, P, wid);
  dma_piece<0>(src, ldsw, 0); dma_piece<4>(src, ldsw, 0); dma_piece<1>(src, ldsw, 0); dma_piece<5>(src, ldsw, 0); dma_piece<2>(src, ldsw, 0); dma_piece<6>(src, ldsw, 0); dma_piece<3>(src, ldsw, 0); dma_piece<7>(src, ldsw, 0); src_next(src);
  dma_piece<0>(src, ldsw, 1); dma_piece<4>(src, ldsw, 1); dma_piece<1>(src, ldsw, 1); dma_piece<5>(src, ldsw, 1); dma_piece<2>(src, ldsw, 1); dma_piece<6>(src, ldsw, 1); dma_piece<3>(src, ldsw, 1); dma_piece<7>(src, ldsw, 1); src_next(src);
  Warm wm; setup_warm(wm, u0, true, 2, P, wid, (unsigned)(uintptr_t)lds + LDS_T + (unsigned)wid * 256u);
  f32x4 acc[8][4]; Adr ad;
  if (u0.kind == U_B && wid < 4) dma16((unsigned)(uintptr_t)lds + WPHI_OFF + (unsigned)wid * 1024u, (unsigned)lane * 16u, mk_rsrc(wphi + u0.pn * 1600, 3200u), (unsigned)wid * 1024u);
  Unit u = u0;
  asm volatile("s_waitcnt vmcnt(8)\n\ts_barrier" ::: "memory");
  SBAR();
#pragma unroll
  for (int i = 0; i < BULK_N; ++i) bulk_touch((int)blockIdx.x * 8 + wid + i * 2048, P, wo, Bwo, (unsigned)(uintptr_t)lds + LDS_T + (unsigned)wid * 256u, lane);

  SBAR();
  for (int ui = 0; ui < 2; ++ui) {
    const bool has_next = two && ui == 0;
    const int nt = (u.kind == U_K || u.kind == U_V) ? 8 : 10;
    setup_adr(ad, lds, u.kind == U_K || u.kind == U_V, wr, wc);
    if (u.kind == U_V) run_unit<true>(acc, ad, src, wm, u1, P, wid, has_next, nt, ldsw, u, Qf, Kf, Vf);
    else run_unit<false>(acc, ad, src, wm, u1, P, wid, has_next, nt, ldsw, u, Qf, Kf, Vf);
    if (!has_next && j >= 16 && j < 24) {
      asm volatile("s_waitcnt vmcnt(0) lgkmcnt(0)\n\ts_barrier" ::: "memory");
      relayout_issue(wo, Bwo, lds, wid, (x * 8 + (j - 16)) * 8 + wid);
    }
    if (u.kind == U_B) {
      unsigned long long bts[4] = {};
      epi_b(acc, u, lds, PhiQ, Part, wid, bts);
      BSTAMP(2);
    }
    if (!has_next) break;
    u = u1;
  }
  if (j >= 16 && j < 24) {
    relayout_finish(WfA, WfB, lds, wid, (x * 8 + (j - 16)) * 8 + wid);
    if (blockIdx.x == 128 && wid < 4) flags[(wid * 64 + fresh_lane()) * 16] = 0u;
  }
  asm volatile("s_waitcnt vmcnt(0)" ::: "memory");
}
}

#define MFMASUM 1
#define CF_AUX 16
namespace attn {
constexpr int LDS_K = 0, LDS_V = 65536, LDS_X = 131072, LDS_BYTES = 163840;
constexpr float L_LIMIT = 1.2676506e30f;

struct Til { f32x4 s[8]; };
struct St {
  f32x4 o[16];
  bf16x8 q[8];
  f32x4 negm[2];
  bf16x8 fr[8];
  u32x4 pw[4];
  f32x2 l[2];
  f32x4 ls[2]; bf16x8 ones;
  float m[2];
};

DI void cmask(Til& t, int thr) {
  const float NEG = -__builtin_inff();
#pragma unroll
  for (int kb = 0; kb < 4; ++kb)
#pragma unroll
    for (int qb = 0; qb < 2; ++qb)
#pragma unroll
      for (int r = 0; r < 4; ++r) if (32 * (kb >> 1) + 4 * (kb & 1) + r - 16 * qb > thr) t.s[2 * kb + qb][r] = NEG;
}

DI void safe_path(const u16* __restrict__ Qf, const u16* __restrict__ Kf, const u16* __restrict__ Vf, u16* __restrict__ Cf, int head, int blk, int rg, int grp, int lane) {
  const int g = lane >> 4, c = lane & 15;
  for (int qb = 0; qb < 2; ++qb) {
    const bf16x8* qp = (const bf16x8*)Qf + ((size_t)(head * 64 + blk * 4 + rg) * 8 + qb * 4) * 64 + lane;
    bf16x8 q[4];
#pragma unroll
    for (int d = 0; d < 4; ++d) q[d] = qp[d * 64];
    f32x4 o[4] = {};
    float m = -1e30f, l = 0.f;
    const int row = blk * 128 + rg * 32 + qb * 16 + c;
    const int nsteps = blk * 4 + rg + 1;
    for (int ks = 0; ks < nsteps; ++ks) {
      const bf16x8* kp = (const bf16x8*)Kf + ((size_t)(head * 32 + (ks >> 1)) * 16 + (ks & 1) * 8) * 64 + lane;
      f32x4 s[2] = {};
      float mx = -__builtin_inff();
#pragma unroll
      for (int b = 0; b < 2; ++b) {
#pragma unroll
        for (int d = 0; d < 4; ++d) s[b] = MFMA16(kp[(b * 4 + d) * 64], q[d], s[b]);
#pragma unroll
        for (int r = 0; r < 4; ++r) { if (ks * 32 + 8 * g + 4 * b + r > row) s[b][r] = -__builtin_inff(); mx = fmaxf(mx, s[b][r]); }
      }
      mx = xrow_max(mx);
      const float mn = fmaxf(m, mx), al = __builtin_amdgcn_exp2f(m - mn); m = mn;
      float ps = 0.f;
#pragma unroll
      for (int b = 0; b < 2; ++b)
#pragma unroll
        for (int r = 0; r < 4; ++r) { s[b][r] = __builtin_amdgcn_exp2f(s[b][r] - mn); ps += s[b][r]; }
      l = l * al + ps;
      const u32x4 w = {cvtpk(s[0][0], s[0][1]), cvtpk(s[0][2], s[0][3]), cvtpk(s[1][0], s[1][1]), cvtpk(s[1][2], s[1][3])};
      const bf16x8* vp = (const bf16x8*)Vf + ((size_t)(head * 32 + (ks >> 1)) * 16 + (ks & 1) * 8 + grp * 4) * 64 + lane;
#pragma unroll
      for (int db = 0; db < 4; ++db) { o[db] *= al; o[db] = MFMA16(vp[db * 64], __builtin_bit_cast(bf16x8, w), o[db]); }
    }
    const float il = 1.f / xrow_sum(l);
    u32x4* cp = (u32x4*)Cf + ((size_t)(head * 4 + grp * 2) * 128 + blk * 8 + rg * 2 + qb) * 64 + (c + 16 * (((g & 1) << 1) | (g >> 1)));
#pragma unroll
    for (int t = 0; t < 2; ++t)
      cp[t * 128 * 64] = pair_swap(cvtpk(o[2 * t][0] * il, o[2 * t][1] * il), cvtpk(o[2 * t][2] * il, o[2 * t][3] * il), cvtpk(o[2 * t + 1][0] * il, o[2 * t + 1][1] * il), cvtpk(o[2 * t + 1][2] * il, o[2 * t + 1][3] * il));
  }
}

#define A_RDK(n) (st.fr[(n) & 7] = lds_rd128(kp + (n) * 1024))
#define A_RDV(n) (st.fr[(n) & 7] = lds_rd128(vp + (n) * 1024))
#define RDK2(a, b) do { A_RDK(a); SBAR(); A_RDK(b); } while (0)
#define RDV2(a, b) do { A_RDV(a); SBAR(); A_RDV(b); } while (0)
#define EXP2(x) __builtin_amdgcn_exp2f(x)

#define SUMS(qb, a, b) do { } while (0)
struct Dma {
  unsigned voff; rsrc_t srdK, srdV; unsigned kdst, vdst, ksoff, vsoff; };
#define DMA_PIECE(d, j) do { if ((j) == 0) set_m0((d).kdst); if ((j) == 4) set_m0((d).vdst); \
    if ((j) < 4) dma16m<((j) & 3) * 1024>((d).voff, (d).srdK, (d).ksoff); else dma16m<((j) & 3) * 1024>((d).voff, (d).srdV, (d).vsoff); } while (0)
#define XBARL() XBARN(0)
#define XBARN(N) do { SBAR(); asm volatile("s_waitcnt vmcnt(" #N ") lgkmcnt(0)\n\ts_barrier" ::: "memory"); SBAR(); } while (0)
#define XBAR() XBARN(0)

struct Ctx {
  const u16 *Qf, *Kf, *Vf; u16* Cf;
  const u16* PhiQ; const float* Part; u16* Of; int pp;
  const LAS char *lds, *kbase, *vbase;
  unsigned lds0, wpiece;
  int lane, wid, grp, rg, head, T1, g;
};
DI void load_q(St& st, const Ctx& c, int blk) {
  const bf16x8* qp = (const bf16x8*)c.Qf + ((size_t)(c.head * 64 + blk * 4 + c.rg) * 8) * 64 + c.lane;
#pragma unroll
  for (int i = 0; i < 8; ++i) st.q[i] = qp[i * 64];
}
#define PIN_Q() asm volatile("" : "+v"(st.q[0]), "+v"(st.q[1]), "+v"(st.q[2]), "+v"(st.q[3]), "+v"(st.q[4]), "+v"(st.q[5]), "+v"(st.q[6]), "+v"(st.q[7]))
DI void dma_advance(Dma& d, const Ctx& c, int gg) {
  d.kdst ^= 32768u; d.vdst ^= 32768u; d.vsoff = d.ksoff;
  d.ksoff = (gg + 2 == c.T1) ? c.wpiece : d.ksoff + 32768u;
}

template <bool FIRST>
DI void phaseA(St& st, Til& C, const Til& P, const LAS char* kp, const LAS char* vp, const Dma& d) {
#define GA(i) do { \
    if (((i) & 3) == 0) { if (FIRST) mfma_zero(C.s[2 * ((i) >> 2)], st.fr[(i) & 7], st.q[0]); else mfma_init(C.s[2 * ((i) >> 2)], st.fr[(i) & 7], st.q[0], st.negm[0]); } \
    else mfma_acc(C.s[2 * ((i) >> 2)], st.fr[(i) & 7], st.q[(i) & 3]); \
    SBAR(); \
    if (!FIRST) {   \
      const float a_ = P.s[2 * (2 * ((i) >> 3) + (((i) & 3) >> 1)) + (((i) >> 2) & 1)][2 * ((i) & 1)], b_ = P.s[2 * (2 * ((i) >> 3) + (((i) & 3) >> 1)) + (((i) >> 2) & 1)][2 * ((i) & 1) + 1]; \
      { unsigned w_ = cvtpk(a_, b_); PIN(w_); st.pw[(i) >> 2][(i) & 3] = w_; } \
      SUMS(((i) >> 2) & 1, a_, b_); SBAR(); } \
    if (((i) & 3) == 0) { if (FIRST) mfma_zero(C.s[2 * ((i) >> 2) + 1], st.fr[(i) & 7], st.q[4]); else mfma_init(C.s[2 * ((i) >> 2) + 1], st.fr[(i) & 7], st.q[4], st.negm[1]); } \
    else mfma_acc(C.s[2 * ((i) >> 2) + 1], st.fr[(i) & 7], st.q[4 + ((i) & 3)]); \
    SBAR(); \
    if (((i) & 1) == 1) { if ((i) < 8) { RDK2(((i) + 8) & 15, ((i) + 7) & 15); } else if (!FIRST) { RDV2(((i) + 8) & 15, ((i) + 7) & 15); } } \
    if (FIRST) { st.o[i] = (f32x4){}; PIN(st.o[i]); } \
    if (!FIRST) { if ((i) == 0) DMA_PIECE(d, 3); if ((i) == 2) DMA_PIECE(d, 4); if ((i) == 4) DMA_PIECE(d, 5); if ((i) == 8) DMA_PIECE(d, 6); if ((i) == 10) DMA_PIECE(d, 7); } \
    SBAR(); } while (0)
  GA(0); GA(1); GA(2); GA(3); GA(4); GA(5); GA(6); GA(7); GA(8); GA(9); GA(10); GA(11); GA(12); GA(13); GA(14); GA(15);
  if (!FIRST) asm volatile("" ::"v"(st.negm[0]), "v"(st.negm[1]));
#undef GA
}

#define MSUM(i) do { if (((i) & 7) == 2) mfma_acc(st.ls[0], st.ones, __builtin_bit_cast(bf16x8, st.pw[2 * ((i) >> 3)])); if (((i) & 7) == 5) mfma_acc(st.ls[1], st.ones, __builtin_bit_cast(bf16x8, st.pw[2 * ((i) >> 3) + 1])); } while (0)
template <class CTX> DI void phaseB(St& st, Til& C, const LAS char* vp, const LAS char* kn, Dma& d, const CTX& c, int g) {
#define GB(i) do { \
    if ((i) == 11) { XBARL(); dma_advance(d, c, g); } \
    mfma_acc(st.o[2 * ((i) & 7)], st.fr[(i) & 7], __builtin_bit_cast(bf16x8, st.pw[2 * ((i) >> 3)])); \
    SBAR(); \
    { float e_ = EXP2(C.s[(i) >> 1][2 * ((i) & 1)]); PIN(e_); C.s[(i) >> 1][2 * ((i) & 1)] = e_; } \
    SBAR(); \
    mfma_acc(st.o[2 * ((i) & 7) + 1], st.fr[(i) & 7], __builtin_bit_cast(bf16x8, st.pw[2 * ((i) >> 3) + 1])); \
    SBAR(); \
    { float e_ = EXP2(C.s[(i) >> 1][2 * ((i) & 1) + 1]); PIN(e_); C.s[(i) >> 1][2 * ((i) & 1) + 1] = e_; } \
    SBAR(); \
    MSUM(i); \
    if (((i) & 1) == 1 && (i) < 8) { RDV2(((i) + 8) & 15, ((i) + 7) & 15); } \
    if ((i) == 11) { { const LAS char* kp = kn; RDK2(1, 0); SBAR(); RDK2(3, 2); } } \
    if ((i) == 12) { DMA_PIECE(d, 0); } \
    if ((i) == 13) { { const LAS char* kp = kn; RDK2(5, 4); } } \
    if ((i) == 14) { DMA_PIECE(d, 1); DMA_PIECE(d, 2); } \
    if ((i) == 15) { { const LAS char* kp = kn; RDK2(7, 6); } } \
    SBAR(); } while (0)
  GB(0); GB(1); GB(2); GB(3); GB(4); GB(5); GB(6); GB(7); GB(8); GB(9); GB(10); GB(11); GB(12); GB(13); GB(14); GB(15);
#undef GB
}

struct BIn { bf16x8 q0[2], q1[2]; };
struct BPart { f32x4 part[8]; };
DI void b2_load_part(BPart& b, const float* __restrict__ Part, int head, int wid, int lane) {
  if (wid < 6) {
#pragma unroll
    for (int pm = 0; pm < 8; ++pm) b.part[pm] = ((const f32x4*)Part)[(((size_t)pm * 32 + head) * 6 + wid) * 64 + lane];
  }
}
DI void b2_load(BIn& b, const u16* __restrict__ PhiQ, int head, int pp, int wid, int lane) {
  const int c = lane & 15, g = lane >> 4;
#pragma unroll
  for (int i = 0; i < 2; ++i) { const char* src = (const char*)PhiQ + ((size_t)head * 128 + pp * 16 + wid * 2 + i) * 1280;
    b.q0[i] = *(const bf16x8*)(src + lane * 16);
    b.q1[i] = *(const bf16x8*)(src + 1024 + c * 16);
    if (g != 0) b.q1[i] = (bf16x8){0, 0, 0, 0, 0, 0, 0, 0}; }
}
DI void b2_publish(const BPart& b, LAS char* xr, int wid, int lane) {
  if (wid < 6) { const f32x4 s = ((b.part[0] + b.part[1]) + (b.part[2] + b.part[3])) + ((b.part[4] + b.part[5]) + (b.part[6] + b.part[7]));
    *(LAS u32x2*)(xr + wid * 512 + lane * 8) = (u32x2){cvtpk(s[0], s[1]), cvtpk(s[2], s[3])}; }
}
DI void b2_finish(const BIn& b, const LAS char* xr, u16* __restrict__ Of, int head, int pp, int wid, int lane) {
  const int c = lane & 15, g = lane >> 4;
  bf16x8 a0[2], a1[2];
#pragma unroll
  for (int db = 0; db < 2; ++db) {
    a0[db] = __builtin_shufflevector(*(const LAS s16x4*)(xr + db * 512 + lane * 8), *(const LAS s16x4*)(xr + (2 + db) * 512 + lane * 8), 0, 1, 2, 3, 4, 5, 6, 7);
    a1[db] = __builtin_shufflevector(*(const LAS s16x4*)(xr + (4 + db) * 512 + c * 8), *(const LAS s16x4*)(xr + (4 + db) * 512 + (c + 16) * 8), 0, 1, 2, 3, 4, 5, 6, 7);
    if (g != 0) a1[db] = (bf16x8){0, 0, 0, 0, 0, 0, 0, 0};
  }
#pragma unroll
  for (int i = 0; i < 2; ++i) {
    f32x4 n0 = MFMA16(a0[0], b.q0[i], ((f32x4){})); n0 = MFMA16(a1[0], b.q1[i], n0);
    f32x4 n1 = MFMA16(a0[1], b.q0[i], ((f32x4){})); n1 = MFMA16(a1[1], b.q1[i], n1);
    const float inv = 1.f / (__shfl(n1[0], c + 16) + 1e-6f);
    char* base = (char*)Of + (size_t)(pp * 16 + wid * 2 + i) * 1024;
    { const int k = head * 20 + 4 * g; *(u32x2*)(base + (size_t)(k >> 5) * 131072 + (c + 16 * ((k >> 3) & 3)) * 16 + 8 * ((k >> 2) & 1)) = (u32x2){cvtpk(n0[0] * inv, n0[1] * inv), cvtpk(n0[2] * inv, n0[3] * inv)}; }
    if (g == 0) { const int k = head * 20 + 16; *(u32x2*)(base + (size_t)(k >> 5) * 131072 + (c + 16 * ((k >> 3) & 3)) * 16 + 8 * ((k >> 2) & 1)) = (u32x2){cvtpk(n1[0] * inv, n1[1] * inv), cvtpk(n1[2] * inv, n1[3] * inv)}; }
  }
}

template <int BI>
DI void do_block(St& st, Dma& d, Ctx& c, const int blk, const int next_blk) {
  const int T = blk + 1, lane = c.lane, grp = c.grp, rg = c.rg, wid = c.wid;
  int g = c.g;
  const int thr = rg * 32 + (lane & 15) - grp * 64 - 8 * (lane >> 4);
  Til a, b;
  { const LAS char* kp = c.kbase + (g & 1) * 32768;
    A_RDK(1); A_RDK(0); A_RDK(3); A_RDK(2); A_RDK(5); A_RDK(4); A_RDK(7); A_RDK(6);
    phaseA<true>(st, a, a, kp, kp, d);
    MFMA_SETTLE();
    if (T == 1) cmask(a, thr);
#pragma unroll
    for (int qb = 0; qb < 2; ++qb) {
      float mx = max3f(a.s[qb][0], a.s[qb][1], a.s[qb][2]), my = max3f(a.s[qb][3], a.s[2 + qb][0], a.s[2 + qb][1]);
      mx = max3f(mx, a.s[2 + qb][2], a.s[2 + qb][3]); my = max3f(my, a.s[4 + qb][0], a.s[4 + qb][1]);
      mx = max3f(mx, a.s[4 + qb][2], a.s[4 + qb][3]); my = max3f(my, a.s[6 + qb][0], a.s[6 + qb][1]);
      mx = max3f(mx, a.s[6 + qb][2], a.s[6 + qb][3]); mx = max3f(mx, my, -1e30f);
      st.m[qb] = xrow_max(mx); st.l[qb] = (f32x2){0.f, 0.f}; st.ls[qb] = (f32x4){};
    }
    *(LAS f32x2*)((LAS char*)c.lds + LDS_X + (wid ^ 4) * 4096 + lane * 8) = (f32x2){st.m[0], st.m[1]};
    XBAR();
    { const f32x2 om = *(const LAS f32x2*)(c.lds + LDS_X + wid * 4096 + lane * 8); st.m[0] = fmaxf(st.m[0], om[0]); st.m[1] = fmaxf(st.m[1], om[1]); }
    dma_advance(d, c, g);
    DMA_PIECE(d, 0); DMA_PIECE(d, 1); DMA_PIECE(d, 2);
    { const LAS char* kp = c.kbase + ((g + 1) & 1) * 32768; A_RDK(1); A_RDK(0); A_RDK(3); A_RDK(2); A_RDK(5); A_RDK(4); A_RDK(7); A_RDK(6); }
#pragma unroll
    for (int qb = 0; qb < 2; ++qb) { st.negm[qb] = (f32x4){-st.m[qb], -st.m[qb], -st.m[qb], -st.m[qb]};
#pragma unroll
      for (int kb = 0; kb < 4; ++kb)
#pragma unroll
        for (int r = 0; r < 4; ++r) a.s[2 * kb + qb][r] = EXP2(a.s[2 * kb + qb][r] - st.m[qb]); }
    asm volatile("s_nop 1" : "+v"(st.negm[0]), "+v"(st.negm[1]), "+v"(st.ls[0]), "+v"(st.ls[1]));
    ++g;
  }
#define STEP(C, P, t, kp_, vp_, kn_) do { \
    phaseA<false>(st, C, P, kp_, vp_, d); \
    if ((t) == T - 1) { MFMA_SETTLE(); cmask(C, thr); } \
    SBAR(); \
    phaseB(st, C, vp_, kn_, d, c, g); \
    ++g; } while (0)
  int t = 1;
  const LAS char* kE = c.kbase + (g & 1) * 32768; const LAS char* kO = c.kbase + ((g + 1) & 1) * 32768;
  const LAS char* vE = c.vbase + ((g + 1) & 1) * 32768; const LAS char* vO = c.vbase + (g & 1) * 32768;
  for (; t + 1 < T; t += 2) { STEP(b, a, t, kE, vE, kO); STEP(a, b, t + 1, kO, vO, kE); }
  if (t < T) { STEP(b, a, t, kE, vE, kO); a = b; }
#undef STEP
  DMA_PIECE(d, 3);
  if (BI == 0) load_q(st, c, next_blk);
  BIn bi;
  BPart bp;
  if (BI == 1) { b2_load_part(bp, c.Part, c.head, wid, lane); b2_load(bi, c.PhiQ, c.head, c.pp, wid, lane); }
  { const LAS char* vp = c.vbase + ((g - 1) & 1) * 32768;
    A_RDV(1); A_RDV(0); A_RDV(3); A_RDV(2); A_RDV(5); A_RDV(4); A_RDV(7); A_RDV(6);
#pragma unroll
    for (int i = 0; i < 16; ++i) { const float x = a.s[2 * (2 * (i >> 3) + ((i & 3) >> 1)) + ((i >> 2) & 1)][2 * (i & 1)], y = a.s[2 * (2 * (i >> 3) + ((i & 3) >> 1)) + ((i >> 2) & 1)][2 * (i & 1) + 1];
      st.pw[i >> 2][i & 3] = cvtpk(x, y);
    }
#define GD(i) do { mfma_acc(st.o[2 * ((i) & 7)], st.fr[(i) & 7], __builtin_bit_cast(bf16x8, st.pw[2 * ((i) >> 3)])); \
      mfma_acc(st.o[2 * ((i) & 7) + 1], st.fr[(i) & 7], __builtin_bit_cast(bf16x8, st.pw[2 * ((i) >> 3) + 1])); MSUM(i); \
      if (((i) & 1) == 1 && (i) < 8) { RDV2(((i) + 8) & 15, ((i) + 7) & 15); } SBAR(); } while (0)
    asm volatile("s_nop 1" : "+v"(st.pw[0]), "+v"(st.pw[1]), "+v"(st.pw[2]), "+v"(st.pw[3]));
    GD(0); GD(1); GD(2); GD(3); GD(4); GD(5); GD(6); GD(7); GD(8); GD(9); GD(10); GD(11); GD(12); GD(13); GD(14); GD(15);
    MFMA_SETTLE();
#undef GD
  }
  const float l[2] = {st.ls[0][0], st.ls[1][0]};
  LAS char* xr = (LAS char*)c.lds + LDS_X;
  LAS char* sendp = xr + wid * 4096 + lane * 16;
  const LAS char* recvp = xr + (wid ^ 4) * 4096 + lane * 16;
  LAS char* vnext = (LAS char*)c.lds + LDS_V + (g & 1) * 32768;
  f32x4 k[8];
#define PACK8(x, y) (u32x4){cvtpk((x)[0], (x)[1]), cvtpk((x)[2], (x)[3]), cvtpk((y)[0], (y)[1]), cvtpk((y)[2], (y)[3])}
  if (grp == 0) {
#pragma unroll
    for (int i = 0; i < 4; ++i) *(LAS u32x4*)(sendp + i * 1024) = PACK8(st.o[8 + 4 * (i >> 1) + (i & 1)], st.o[8 + 4 * (i >> 1) + 2 + (i & 1)]);
#pragma unroll
    for (int i = 0; i < 8; ++i) k[i] = st.o[i];
  } else {
#pragma unroll
    for (int i = 0; i < 4; ++i) *(LAS u32x4*)(sendp + i * 1024) = PACK8(st.o[4 * (i >> 1) + (i & 1)], st.o[4 * (i >> 1) + 2 + (i & 1)]);
#pragma unroll
    for (int i = 0; i < 8; ++i) k[i] = st.o[8 + i];
  }
  *(LAS f32x2*)(vnext + (wid ^ 4) * 4096 + lane * 8) = (f32x2){l[0], l[1]};
  LAS char* bkv = (LAS char*)c.lds + LDS_K + (g & 1) * 32768;
  if (BI == 1) b2_publish(bp, bkv, wid, lane);
  asm volatile("s_waitcnt lgkmcnt(0)\n\ts_barrier" ::: "memory");
  const f32x2 ol = *(const LAS f32x2*)(vnext + wid * 4096 + lane * 8);
  u32x4 xo[4];
#pragma unroll
  for (int i = 0; i < 4; ++i) xo[i] = *(const LAS u32x4*)(recvp + i * 1024);
  asm volatile("s_waitcnt lgkmcnt(0)" : "+v"(xo[0]), "+v"(xo[1]), "+v"(xo[2]), "+v"(xo[3]) :: "memory");
  DMA_PIECE(d, 4); DMA_PIECE(d, 5); DMA_PIECE(d, 6); DMA_PIECE(d, 7);
  bool bad = !(l[0] < L_LIMIT) || !(l[1] < L_LIMIT) || !(ol[0] < L_LIMIT) || !(ol[1] < L_LIMIT);
  const float fs[2] = {__builtin_amdgcn_rcpf(l[0] + ol[0]), __builtin_amdgcn_rcpf(l[1] + ol[1])};
  f32x2 chk = {0.f, 0.f};
  u32x4 res[4];
#pragma unroll
  for (int i = 0; i < 4; ++i) {
    const u32x4 x = xo[i];
    const f32x4 p = k[4 * (i >> 1) + (i & 1)], q = k[4 * (i >> 1) + 2 + (i & 1)]; const float f = fs[i & 1];
    const float v0 = (p[0] + bf_lo(x[0])) * f, v1 = (p[1] + bf_hi(x[0])) * f, v2 = (p[2] + bf_lo(x[1])) * f, v3 = (p[3] + bf_hi(x[1])) * f;
    const float v4 = (q[0] + bf_lo(x[2])) * f, v5 = (q[1] + bf_hi(x[2])) * f, v6 = (q[2] + bf_lo(x[3])) * f, v7 = (q[3] + bf_hi(x[3])) * f;
    chk = __builtin_elementwise_fma((f32x2){v0, v1}, (f32x2){0.f, 0.f}, chk); chk = __builtin_elementwise_fma((f32x2){v2, v3}, (f32x2){0.f, 0.f}, chk);
    chk = __builtin_elementwise_fma((f32x2){v4, v5}, (f32x2){0.f, 0.f}, chk); chk = __builtin_elementwise_fma((f32x2){v6, v7}, (f32x2){0.f, 0.f}, chk);
    res[i] = pair_swap(cvtpk(v0, v1), cvtpk(v2, v3), cvtpk(v4, v5), cvtpk(v6, v7));
  }
  bad = bad || !(chk[0] == 0.f) || !(chk[1] == 0.f);
  if (__builtin_expect(__any(bad), 0)) {
    safe_path(c.Qf, c.Kf, c.Vf, c.Cf, c.head, blk, rg, grp, lane);
  } else {
#pragma unroll
    for (int i = 0; i < 4; ++i) st128<CF_AUX>(res[i], mk_rsrc(c.Cf, 2048u * 4096u * 2u), (unsigned)(((c.head * 4 + grp * 2 + (i >> 1)) * 128 + blk * 8 + rg * 2 + (i & 1)) * 64 + ((lane & 15) + 16 * ((((lane >> 4) & 1) << 1) | (lane >> 5)))) * 16u);
  }
  if (BI == 1) b2_finish(bi, bkv, c.Of, c.head, c.pp, wid, lane);
  c.g = g;
}

__global__ __launch_bounds__(512, 2) void attn_kernel(const u16* __restrict__ Qf, const u16* __restrict__ Kf, const u16* __restrict__ Vf, u16* __restrict__ Cf,
                                                      const u16* __restrict__ PhiQ, const float* __restrict__ Part, u16* __restrict__ Of, unsigned long long* dbg) {
  extern __shared__ __attribute__((aligned(16))) char smem[];
  Ctx c; c.Qf = Qf; c.Kf = Kf; c.Vf = Vf; c.Cf = Cf; c.PhiQ = PhiQ; c.Part = Part; c.Of = Of;
  c.lds = (const LAS char*)smem; c.lds0 = (unsigned)(uintptr_t)c.lds;
  const int tid = threadIdx.x; c.lane = tid & 63;
  c.wid = (int)rfl(tid >> 6); c.grp = c.wid >> 2; c.rg = c.wid & 3;
  const int xcd = blockIdx.x & 7, jj = blockIdx.x >> 3, pp = jj & 7; c.head = xcd * 4 + (jj >> 3);
  c.T1 = 16 - pp; c.g = 0; c.pp = pp;
  c.wpiece = (unsigned)c.wid * 4096u;
  c.kbase = c.lds + LDS_K + c.grp * 16384 + c.lane * 16; c.vbase = c.lds + LDS_V + c.grp * 16384 + c.lane * 16;
  Dma d;
  d.voff = (unsigned)c.lane * 16u;
  d.srdK = mk_rsrc(Kf + (size_t)c.head * 262144, 524288u); d.srdV = mk_rsrc(Vf + (size_t)c.head * 262144, 524288u);
  St st;
  st.ones = (bf16x8){0x3F80, 0x3F80, 0x3F80, 0x3F80, 0x3F80, 0x3F80, 0x3F80, 0x3F80}; PIN(st.ones);
  d.kdst = c.lds0 + LDS_K + c.wpiece; d.ksoff = c.wpiece; d.vdst = c.lds0 + LDS_V + c.wpiece; d.vsoff = c.wpiece;
  DMA_PIECE(d, 0); DMA_PIECE(d, 1); DMA_PIECE(d, 2); DMA_PIECE(d, 3);
  load_q(st, c, 15 - pp);
  PIN_Q();
  XBAR();
  DMA_PIECE(d, 4); DMA_PIECE(d, 5); DMA_PIECE(d, 6); DMA_PIECE(d, 7);
  d.kdst = c.lds0 + LDS_K + 32768 + c.wpiece; d.ksoff = 32768u + c.wpiece;
  DMA_PIECE(d, 0); DMA_PIECE(d, 1); DMA_PIECE(d, 2); DMA_PIECE(d, 3);
  do_block<0>(st, d, c, 15 - pp, pp);
  PIN_Q();
  do_block<1>(st, d, c, pp, pp);
  asm volatile("s_waitcnt vmcnt(0)" ::: "memory");
}
}

namespace outp {
constexpr int SLOTB = 40960, LDS_BYTES = 4 * SLOTB;
struct Src { rsrc_t X, W; unsigned sX, sW; };
struct XSet { bf16x8 f[2][2]; };
struct WSet { bf16x8 f[10]; };
DI void ldx(bf16x8& d, unsigned voff, rsrc_t r, unsigned soff) { asm volatile("buffer_load_dwordx4 %0, %1, %2, %3 offen" : "=v"(d) : "v"(voff), "s"(r), "s"(soff) : "memory"); }
DI void ldx1k(bf16x8& d, unsigned voff, rsrc_t r, unsigned soff) { asm volatile("buffer_load_dwordx4 %0, %1, %2, %3 offen offset:1024" : "=v"(d) : "v"(voff), "s"(r), "s"(soff) : "memory"); }
#define RW(set, p, ks, n) (set.f[n] = lds_rd128((p) + (ks) * 10240 + (n) * 1024))
template <int I> DI void wp(const Src& s, unsigned ldsw, unsigned voff) {
  if constexpr (I == 0 || I == 2) set_m0(ldsw);
  if constexpr (I == 4) set_m0(ldsw + 4096);
  dma16m<(I & 3) * 1024>(voff, s.W, s.sW + (I >> 2) * 4096u);
}
DI void round(f32x4 (&acc)[2][10], XSet& X, WSet& wA, WSet& wB, const LAS char* wc, const LAS char* wn_, Src& src, const Src& srcA, unsigned lw2, unsigned lw3, unsigned voff,
              bool h2, bool h3, int wait, bool swx, bool sww) {
#define MM(k, W_, m, n) do { mfma_acc(acc[m][n], W_.f[n], X.f[k][m]); SBAR(); } while (0)
#define HX(k, m) do { if (h2) { if (m) ldx1k(X.f[k][1], voff, src.X, src.sX + (k) * 262144u); else ldx(X.f[k][0], voff, src.X, src.sX + (k) * 262144u); } SBAR(); } while (0)
#define HW2(i) do { if (h2) wp<i>(src, lw2, voff); SBAR(); } while (0)
#define HW3(i) do { if (h3) wp<i>(src, lw3, voff); SBAR(); } while (0)
#define NOP_() do { } while (0)
#define KSTEP(k, CUR, NXT, p, A, B, C, D) do { \
    MM(k, CUR, 0, 0); RW(NXT, p, 0, 0); SBAR(); MM(k, CUR, 1, 0); MM(k, CUR, 0, 1); RW(NXT, p, 0, 1); SBAR(); MM(k, CUR, 1, 1); A; MM(k, CUR, 0, 2); RW(NXT, p, 0, 2); SBAR(); MM(k, CUR, 1, 2); \
    MM(k, CUR, 0, 3); RW(NXT, p, 0, 3); SBAR(); MM(k, CUR, 1, 3); B; MM(k, CUR, 0, 4); RW(NXT, p, 0, 4); SBAR(); MM(k, CUR, 1, 4); MM(k, CUR, 0, 5); RW(NXT, p, 0, 5); SBAR(); MM(k, CUR, 1, 5); C; \
    MM(k, CUR, 0, 6); RW(NXT, p, 0, 6); SBAR(); MM(k, CUR, 1, 6); MM(k, CUR, 0, 7); RW(NXT, p, 0, 7); SBAR(); MM(k, CUR, 1, 7); D; MM(k, CUR, 0, 8); RW(NXT, p, 0, 8); SBAR(); MM(k, CUR, 1, 8); \
    MM(k, CUR, 0, 9); RW(NXT, p, 0, 9); SBAR(); MM(k, CUR, 1, 9); } while (0)
  if (swx) src.sX = srcA.sX, src.X = srcA.X;
  KSTEP(0, wA, wB, wc + 20480, HW2(2), HW2(3), HW2(4), NOP_());
  HX(0, 0);
  if (h2) src.sW += 4u * 40960u;
  if (wait == 8) asm volatile("s_waitcnt vmcnt(8) lgkmcnt(0)\n\ts_barrier" ::: "memory");
  else if (wait == 6) asm volatile("s_waitcnt vmcnt(6) lgkmcnt(0)\n\ts_barrier" ::: "memory");
  else asm volatile("s_waitcnt vmcnt(0) lgkmcnt(0)\n\ts_barrier" ::: "memory");
  SBAR();
  if (sww) src.sW = srcA.sW, src.W = srcA.W;
  KSTEP(1, wB, wA, wn_, HX(0, 1), HW3(0), HW3(1), NOP_());
  HX(1, 0); HX(1, 1);
  if (h2) src.sX += 4u * 131072u;
#undef NOP_
#undef HW3
#undef HW2
#undef HX
#undef KSTEP
#undef MM
}
#define PIN_X(XS) asm volatile("" : "+v"(XS.f[0][0]), "+v"(XS.f[0][1]), "+v"(XS.f[1][0]), "+v"(XS.f[1][1]))
#define PIN_ACC() do { asm volatile("" : "+v"(acc[0][0]), "+v"(acc[0][1]), "+v"(acc[0][2]), "+v"(acc[0][3]), "+v"(acc[0][4]), "+v"(acc[0][5]), "+v"(acc[0][6]), "+v"(acc[0][7]), "+v"(acc[0][8]), "+v"(acc[0][9])); \
    asm volatile("s_nop 1" : "+v"(acc[1][0]), "+v"(acc[1][1]), "+v"(acc[1][2]), "+v"(acc[1][3]), "+v"(acc[1][4]), "+v"(acc[1][5]), "+v"(acc[1][6]), "+v"(acc[1][7]), "+v"(acc[1][8]), "+v"(acc[1][9])); SBAR(); } while (0)
#define RLX_AGENT __ATOMIC_RELAXED, __HIP_MEMORY_SCOPE_AGENT
DI u32x2 pack4(const f32x4& a) { return (u32x2){cvtpk(a[0], a[1]), cvtpk(a[2], a[3])}; }
DI f32x4 unpack4(unsigned x, unsigned y) { return (f32x4){bf_lo(x), bf_hi(x), bf_lo(y), bf_hi(y)}; }

__global__ __launch_bounds__(512, 2) void outp_kernel(const u16* __restrict__ Cf, const u16* __restrict__ Of, const u16* __restrict__ WfA, const u16* __restrict__ WfB,
                                                      u16* __restrict__ out, u16* __restrict__ Ex, unsigned* __restrict__ flags, unsigned long long* dbg) {
#define OST(i)
  extern __shared__ __attribute__((aligned(16))) char smem[];
  const LAS char* lds = (const LAS char*)smem;
  const int tid = threadIdx.x, lane = tid & 63, wid = (int)rfl(tid >> 6), par = wid >> 2, wm = wid & 3;
  const int x = blockIdx.x & 7, j = blockIdx.x >> 3, kq = x >> 1, pm = 8 * (x & 1) + (j >> 2), pn = j & 3, tile = pm * 4 + pn;
  const unsigned ldsw = (unsigned)(uintptr_t)lds + (unsigned)wid * 5120u, voff = (unsigned)lane * 16u;
  const int a0 = kq == 0 ? 0 : (kq == 1 ? 3 : (kq == 2 ? 13 : 23)), R = kq == 0 ? 8 : (kq == 3 ? 9 : 10);
  const unsigned ox = (unsigned)(par * 128 + pm * 8 + wm * 2) * 1024u, ow = (unsigned)((wid >> 1) * 40 + pn * 10 + (wid & 1) * 5) * 1024u;
  Src srcA, src;
  srcA.X = mk_rsrc(Cf, 2048u * 4096u * 2u); srcA.W = mk_rsrc(WfA, 4096u * 640u * 2u);
  srcA.sX = (unsigned)(a0 * 4 * 128) * 1024u + ox; srcA.sW = (unsigned)(a0 * 4 * 40) * 1024u + ow;
  src.X = mk_rsrc(kq == 0 ? Of : Cf, kq == 0 ? 2048u * 640u * 2u : 2048u * 4096u * 2u); src.W = mk_rsrc(kq == 0 ? WfB : WfA, kq == 0 ? 640u * 640u * 2u : 4096u * 640u * 2u);
  src.sX = kq == 0 ? ox : srcA.sX; src.sW = kq == 0 ? ow : srcA.sW;
  XSet X0, X1;
#define FETCH(XS, slot) do { _Pragma("unroll") for (int k = 0; k < 2; ++k) { ldx(XS.f[k][0], voff, src.X, src.sX + k * 262144u); ldx1k(XS.f[k][1], voff, src.X, src.sX + k * 262144u); } \
    wp<0>(src, ldsw + (slot) * SLOTB, voff); wp<1>(src, ldsw + (slot) * SLOTB, voff); wp<2>(src, ldsw + (slot) * SLOTB, voff); wp<3>(src, ldsw + (slot) * SLOTB, voff); wp<4>(src, ldsw + (slot) * SLOTB, voff); \
    src.sX += 4u * 131072u; src.sW += 4u * 40960u; } while (0)
  FETCH(X0, 0); FETCH(X1, 1);
#undef FETCH
  wp<0>(src, ldsw + 2 * SLOTB, voff); wp<1>(src, ldsw + 2 * SLOTB, voff);
  const LAS char* wa = lds + par * 10240 + lane * 16;
  f32x4 acc[2][10];
#pragma unroll
  for (int m = 0; m < 2; ++m)
#pragma unroll
    for (int n = 0; n < 10; ++n) acc[m][n] = (f32x4){};
  PIN_ACC();
  WSet wA, wB;
  asm volatile("s_waitcnt vmcnt(11)\n\ts_barrier" ::: "memory"); SBAR();
  OST(0);
#pragma unroll
  for (int n = 0; n < 10; ++n) RW(wA, wa, 0, n);
  const int c = lane & 15, g = lane >> 4;
  const rsrc_t rout = mk_rsrc(out, 2048u * 1280u * 2u);
  const unsigned vout = (unsigned)((pm * 128 + wm * 32 + par * 16 + c) * 2560 + (pn * 160 + 4 * g) * 2);
  u32x2 bres[10];
#pragma unroll
  for (int t = 0; t < 10; ++t) bres[t] = (u32x2){0u, 0u};
  unsigned o0 = 0, o1 = SLOTB, o2 = 2 * SLOTB;
  for (int s = 0; s < R; s += 2) {
    PIN_X(X0);
    round(acc, X0, wA, wB, wa + o0, wa + o1, src, srcA, ldsw + o2, ldsw + o0, voff, s + 2 < R, s + 3 < R, s == 0 ? 6 : (s + 2 < R ? 8 : 0), false, kq == 0 && s == 2);
    if (kq == 0 && s == 4) {
      MFMA_SETTLE();
      LAS char* mw = (LAS char*)lds + 3 * SLOTB + wid * 5120 + lane * 8; const LAS char* mr = lds + 3 * SLOTB + (wid ^ 4) * 5120 + lane * 8;
#pragma unroll
      for (int n = 0; n < 10; ++n) *(LAS u32x2*)(mw + n * 512) = pack4(par == 0 ? acc[1][n] : acc[0][n]);
      asm volatile("s_waitcnt lgkmcnt(0)\n\ts_barrier" ::: "memory");
#pragma unroll
      for (int n = 0; n < 10; ++n) { const u32x2 o = *(const LAS u32x2*)(mr + n * 512); bres[n] = pack4((par == 0 ? acc[0][n] : acc[1][n]) + unpack4(o[0], o[1])); acc[0][n] = (f32x4){}; acc[1][n] = (f32x4){}; }
      PIN_ACC();
    }
    if (s + 1 < R) {
      PIN_X(X1);
      round(acc, X1, wA, wB, wa + o1, wa + o2, src, srcA, ldsw + o0, ldsw + o1, voff, s + 3 < R, s + 4 < R, s + 3 < R ? 8 : 0, kq == 0 && s == 2, false);
    }
    { const unsigned t = o0; o0 = o2; o2 = o1; o1 = t; }
  }
  MFMA_SETTLE();
  OST(1);
  f32x4 own[10];
  asm volatile("s_barrier" ::: "memory");
  { LAS char* mw = (LAS char*)lds + wid * 10240 + lane * 16; const LAS char* mr = lds + (wid ^ 4) * 10240 + lane * 16;
#pragma unroll
    for (int n = 0; n < 10; ++n) { *(LAS f32x4*)(mw + n * 1024) = par == 0 ? acc[1][n] : acc[0][n]; own[n] = par == 0 ? acc[0][n] : acc[1][n]; }
    asm volatile("s_waitcnt lgkmcnt(0)\n\ts_barrier" ::: "memory");
#pragma unroll
    for (int n = 0; n < 10; ++n) own[n] += *(const LAS f32x4*)(mr + n * 1024);
  }
  const rsrc_t rex = mk_rsrc(Ex, 64u * 4u * 8u * 4u * 2048u);
  { const unsigned sb = (unsigned)(((tile * 4 + kq) * 8 + wid) * 4) * 2048u;
#define SEND(o) do { if (kq != (o)) { const u32x2 p0 = pack4(own[o]), p1 = pack4(own[(o) + 4]); st128<16>((u32x4){p0[0], p0[1], p1[0], p1[1]}, rex, sb + (o) * 2048u + lane * 16u); \
      if ((o) < 2) st64<16>(pack4(own[((o) + 8) % 10]), rex, sb + (o) * 2048u + 1024u + lane * 8u); } } while (0)
    SEND(0); SEND(1); SEND(2); SEND(3);
#undef SEND
  }
  asm volatile("s_waitcnt vmcnt(0)\n\ts_barrier" ::: "memory");
  OST(2);
  if (tid == 0) __hip_atomic_store(flags + (tile * 4 + kq) * 16, 1u, RLX_AGENT);
  if (kq == 0) {
#pragma unroll
    for (int n = 0; n < 10; ++n) st64<0>(bres[n], rout, vout + 1280u + n * 32u);
  }
  if (wid == 0) {
    const unsigned long long t0 = __builtin_amdgcn_s_memrealtime();
    for (;;) {
      const unsigned v = lane < 4 ? __hip_atomic_load(flags + (tile * 4 + lane) * 16, RLX_AGENT) : 1u;
      if (__all(v == 1u)) break;
      if (__builtin_amdgcn_s_memrealtime() - t0 > 200000ull) break;
      __builtin_amdgcn_s_sleep(1);
    }
  }
  asm volatile("s_barrier" ::: "memory");
  OST(3);
  u32x4 rp[3]; u32x2 rs[3];
#pragma unroll
  for (int i = 0; i < 3; ++i) { const int src_ = (kq + 1 + i) & 3; const unsigned sb = (unsigned)(((tile * 4 + src_) * 8 + wid) * 4 + kq) * 2048u;
    rp[i] = __builtin_amdgcn_raw_buffer_load_b128(rex, sb + lane * 16u, 0, 16);
    rs[i] = __builtin_amdgcn_raw_buffer_load_b64(rex, kq < 2 ? sb + 1024u + lane * 8u : 0x80000000u, 0, 16); }
  f32x4 f0 = kq == 0 ? own[0] : kq == 1 ? own[1] : kq == 2 ? own[2] : own[3], f1 = kq == 0 ? own[4] : kq == 1 ? own[5] : kq == 2 ? own[6] : own[7], f2 = kq == 0 ? own[8] : own[9];
#pragma unroll
  for (int i = 0; i < 3; ++i) { f0 += unpack4(rp[i][0], rp[i][1]); f1 += unpack4(rp[i][2], rp[i][3]); f2 += unpack4(rs[i][0], rs[i][1]); }
  st64<0>(pack4(f0), rout, vout + (unsigned)kq * 32u); st64<0>(pack4(f1), rout, vout + (unsigned)(kq + 4) * 32u); st64<0>(pack4(f2), rout, kq < 2 ? vout + (unsigned)(kq + 8) * 32u : 0x80000000u);
}
}

extern "C" void kernel_launch(void* const* d_in, const int* in_sizes, int n_in, void* d_out, int out_size, void* d_ws, size_t ws_size, hipStream_t stream) {
  const u16* Ax = (const u16*)d_in[0]; const u16* ckv = (const u16*)d_in[1]; const u16* wq = (const u16*)d_in[2]; const u16* wuk = (const u16*)d_in[3];
  const u16* wuv = (const u16*)d_in[4]; const u16* wo = (const u16*)d_in[5]; const u16* Bx = (const u16*)d_in[6]; const u16* wqkv = (const u16*)d_in[7];
  const u16* wphi = (const u16*)d_in[8]; const u16* Bwo = (const u16*)d_in[9]; u16* out = (u16*)d_out;
  char* ws = (char*)d_ws;
  u16* Qf = (u16*)(ws); u16* Kf = (u16*)(ws + (16u << 20)); u16* Vf = (u16*)(ws + (32u << 20)); u16* Cf = (u16*)(ws + (48u << 20));
  u16* PhiQ = (u16*)(ws + (64u << 20)); u16* Of = (u16*)(ws + (72u << 20)); float* Part = (float*)(ws + (76u << 20));
  u16* WfA = (u16*)(ws + (80u << 20)); u16* WfB = (u16*)(ws + (86u << 20)); unsigned* flags = (unsigned*)(ws + (88u << 20)); u16* Ex = (u16*)(ws + (90u << 20));
  (void)hipFuncSetAttribute((const void*)proj::proj_kernel, hipFuncAttributeMaxDynamicSharedMemorySize, proj::LDS_BYTES);
  (void)hipFuncSetAttribute((const void*)attn::attn_kernel, hipFuncAttributeMaxDynamicSharedMemorySize, attn::LDS_BYTES);
  (void)hipFuncSetAttribute((const void*)outp::outp_kernel, hipFuncAttributeMaxDynamicSharedMemorySize, outp::LDS_BYTES);
  proj::proj_kernel<<<256, 512, proj::LDS_BYTES, stream>>>(Ax, ckv, wq, wuk, wuv, Bx, wqkv, Qf, Kf, Vf, wphi, PhiQ, Part, wo, Bwo, WfA, WfB, flags, nullptr);
  attn::attn_kernel<<<256, 512, attn::LDS_BYTES, stream>>>(Qf, Kf, Vf, Cf, PhiQ, Part, Of, nullptr);
  outp::outp_kernel<<<256, 512, outp::LDS_BYTES, stream>>>(Cf, Of, WfA, WfB, out, Ex, flags, nullptr);
}
```

```cpp
#include <hip/hip_runtime.h>
#include <stdint.h>
typedef unsigned short u16;
typedef short bf16x8 __attribute__((ext_vector_type(8)));
typedef short s16x4 __attribute__((ext_vector_type(4)));
typedef float f32x4 __attribute__((ext_vector_type(4)));
typedef float f32x2 __attribute__((ext_vector_type(2)));
typedef __bf16 bf16x2_t __attribute__((ext_vector_type(2)));
typedef unsigned u32x4 __attribute__((ext_vector_type(4)));
typedef unsigned u32x2 __attribute__((ext_vector_type(2)));
typedef __amdgpu_buffer_rsrc_t rsrc_t;
#define LAS __attribute__((address_space(3)))
#define VM_PAD ""
#define DI __device__ __forceinline__
#define SBAR() __builtin_amdgcn_sched_barrier(0)
#define PIN(x) asm volatile("" : "+v"(x))
#define MFMA16(a, b, c) __builtin_amdgcn_mfma_f32_16x16x32_bf16(a, b, c, 0, 0, 0)
DI void mfma_acc(f32x4& acc, const bf16x8& a, const bf16x8& b) { asm volatile("v_mfma_f32_16x16x32_bf16 %0, %1, %2, %0" : "+v"(acc) : "v"(a), "v"(b)); }
DI void mfma_init(f32x4& d, const bf16x8& a, const bf16x8& b, const f32x4& c) { asm volatile("v_mfma_f32_16x16x32_bf16 %0, %1, %2, %3" : "=&v"(d) : "v"(a), "v"(b), "v"(c)); }
DI void mfma_zero(f32x4& d, const bf16x8& a, const bf16x8& b) { asm volatile("v_mfma_f32_16x16x32_bf16 %0, %1, %2, 0" : "=&v"(d) : "v"(a), "v"(b)); }
#define MFMA_SETTLE() do { SBAR(); asm volatile("s_nop 7\n\ts_nop 1" ::: "memory"); SBAR(); } while (0)
DI unsigned rfl(unsigned x) { return __builtin_amdgcn_readfirstlane(x); }
DI unsigned cvtpk(float lo, float hi) { f32x2 v = {lo, hi}; bf16x2_t b = __builtin_convertvector(v, bf16x2_t); return __builtin_bit_cast(unsigned, b); }
DI float bf_lo(unsigned w) { return __uint_as_float(w << 16); }
DI float bf_hi(unsigned w) { return __uint_as_float(w & 0xffff0000u); }
DI rsrc_t mk_rsrc(const void* p, unsigned bytes) { return __builtin_amdgcn_make_buffer_rsrc((void*)p, (short)0, (int)bytes, 0x00020000); }
DI bf16x8 lds_rd128(const LAS char* p) { return *(const LAS bf16x8*)p; }
typedef short v4i16_t __attribute__((ext_vector_type(4)));
DI s16x4 lds_rdtr(const LAS char* p) { return __builtin_bit_cast(s16x4, __builtin_amdgcn_ds_read_tr16_b64_v4i16((LAS v4i16_t*)p)); }
DI float max3f(float a, float b, float c) { float r; asm("v_max3_f32 %0, %1, %2, %3" : "=v"(r) : "v"(a), "v"(b), "v"(c)); return r; }
DI float max3abs(float a, float b, float c) { float r; asm("v_max3_f32 %0, %1, |%2|, |%3|" : "=v"(r) : "v"(a), "v"(b), "v"(c)); return r; }
DI float fadd1(float a, float b) { float r; asm("v_add_f32_e32 %0, %1, %2" : "=v"(r) : "v"(a), "v"(b)); return r; }
DI void set_m0(unsigned a) { asm volatile("s_mov_b32 m0, %0\n\ts_nop 0" ::"s"(a) : "memory"); }
template <int OFF> DI void dma16m(unsigned voff, rsrc_t srd, unsigned soff) {
  asm volatile(VM_PAD "buffer_load_dwordx4 %0, %1, %2 offen offset:%c3 lds" ::"v"(voff), "s"(srd), "s"(soff), "i"(OFF) : "memory");
}
template <int OFF> DI void dma16m_dev(unsigned voff, rsrc_t srd, unsigned soff) {
  asm volatile(VM_PAD "buffer_load_dwordx4 %0, %1, %2 offen offset:%c3 sc1 lds" ::"v"(voff), "s"(srd), "s"(soff), "i"(OFF) : "memory");
}
DI bf16x8 ld128_dev(rsrc_t r, unsigned voff) { return __builtin_bit_cast(bf16x8, __builtin_amdgcn_raw_buffer_load_b128(r, voff, 0, 16)); }
#define RLX_AGENT __ATOMIC_RELAXED, __HIP_MEMORY_SCOPE_AGENT
constexpr int FL_EPOCH = 0, FL_P1 = 1, FL_P2 = 257, FL_X = 513, FL_U0 = 1025;
typedef unsigned long long tag_t;
DI void flag_set(tag_t* flags, int line, tag_t tag) { __hip_atomic_store(flags + line * 8, tag, RLX_AGENT); }
DI void flag_wait(const tag_t* flags, int line  , tag_t tag) {
  const unsigned long long t0 = __builtin_amdgcn_s_memrealtime();
  for (;;) {
    const tag_t v = line >= 0 ? __hip_atomic_load(flags + line * 8, RLX_AGENT) : tag;
    if (__all(v == tag)) break;
    if (__builtin_amdgcn_s_memrealtime() - t0 > 200000ull) break;
    __builtin_amdgcn_s_sleep(1);
  }
  asm volatile("" ::: "memory");
}
DI void dma16(unsigned ldsaddr, unsigned voff, rsrc_t srd, unsigned soff) {
  asm volatile(VM_PAD "s_mov_b32 m0, %0\n\ts_nop 0\n\tbuffer_load_dwordx4 %1, %2, %3 offen lds" ::"s"(ldsaddr), "v"(voff), "s"(srd), "s"(soff) : "memory");
}
typedef unsigned srd_t __attribute__((ext_vector_type(4)));
DI srd_t mk_srd(const void* p, unsigned bytes) { const unsigned long long a = (unsigned long long)p; return (srd_t){(unsigned)a, (unsigned)(a >> 32) & 0xffffu, bytes, 0x00020000u}; }
DI void dma16s(unsigned ldsaddr, unsigned voff, srd_t srd, unsigned soff) {
  asm volatile(VM_PAD "s_mov_b32 m0, %0\n\ts_nop 0\n\tbuffer_load_dwordx4 %1, %2, %3 offen lds" ::"s"(ldsaddr), "v"(voff), "s"(srd), "s"(soff) : "memory");
}
DI void touch_lds(unsigned ldsaddr, unsigned voff, rsrc_t srd, unsigned soff) {
  asm volatile(VM_PAD "s_mov_b32 m0, %0\n\ts_nop 0\n\tbuffer_load_dword %1, %2, %3 offen lds" ::"s"(ldsaddr), "v"(voff), "s"(srd), "s"(soff) : "memory");
}
#define BULK_BITS "sc0 sc1"
DI void touch_lds_far(unsigned ldsaddr, unsigned voff, rsrc_t srd, unsigned soff) {
  asm volatile(VM_PAD "s_mov_b32 m0, %0\n\ts_nop 0\n\tbuffer_load_dword %1, %2, %3 offen " BULK_BITS " lds" ::"s"(ldsaddr), "v"(voff), "s"(srd), "s"(soff) : "memory");
}
DI void touch_glb(unsigned ldsaddr, const char* p) {
  asm volatile(VM_PAD "s_mov_b32 m0, %0\n\ts_nop 0\n\tglobal_load_lds_dword %1, off sc0 sc1" ::"s"(ldsaddr), "v"(p) : "memory");
}
template <int AUX> DI void st128(const u32x4& w, rsrc_t r, unsigned voff) { asm volatile("" : "+v"(voff)); __builtin_amdgcn_raw_buffer_store_b128(w, r, voff, 0, AUX); }
template <int AUX> DI void st64(const u32x2& w, rsrc_t r, unsigned voff) { asm volatile("" : "+v"(voff)); __builtin_amdgcn_raw_buffer_store_b64(w, r, voff, 0, AUX); }
DI float xrow_max(float x) {
  auto a = __builtin_amdgcn_permlane32_swap(__float_as_uint(x), __float_as_uint(x), false, false); x = fmaxf(__uint_as_float(a[0]), __uint_as_float(a[1]));
  auto b = __builtin_amdgcn_permlane16_swap(__float_as_uint(x), __float_as_uint(x), false, false); return fmaxf(__uint_as_float(b[0]), __uint_as_float(b[1]));
}
DI float xrow_sum(float x) {
  auto a = __builtin_amdgcn_permlane32_swap(__float_as_uint(x), __float_as_uint(x), false, false); x = __uint_as_float(a[0]) + __uint_as_float(a[1]);
  auto b = __builtin_amdgcn_permlane16_swap(__float_as_uint(x), __float_as_uint(x), false, false); return __uint_as_float(b[0]) + __uint_as_float(b[1]);
}
template <bool MAXOP> DI float xop(float a, float b) { if (MAXOP) return __builtin_amdgcn_fmed3f(a, b, __builtin_inff()); else return a + b; }
template <bool MAXOP> DI void xrow4(float& a, float& b, float& c, float& d) {
  auto x = __builtin_amdgcn_permlane32_swap(__float_as_uint(a), __float_as_uint(b), false, false); const float u = xop<MAXOP>(__uint_as_float(x[0]), __uint_as_float(x[1]));
  auto y = __builtin_amdgcn_permlane32_swap(__float_as_uint(c), __float_as_uint(d), false, false); const float v = xop<MAXOP>(__uint_as_float(y[0]), __uint_as_float(y[1]));
  auto z = __builtin_amdgcn_permlane16_swap(__float_as_uint(u), __float_as_uint(v), false, false); const float w = xop<MAXOP>(__uint_as_float(z[0]), __uint_as_float(z[1]));
  auto s = __builtin_amdgcn_permlane32_swap(__float_as_uint(w), __float_as_uint(w), false, false);
  auto p = __builtin_amdgcn_permlane16_swap(s[0], s[0], false, false); auto q = __builtin_amdgcn_permlane16_swap(s[1], s[1], false, false);
  a = __uint_as_float(p[0]); c = __uint_as_float(p[1]); b = __uint_as_float(q[0]); d = __uint_as_float(q[1]);
}
DI float min3f(float a, float b, float c) { float r; asm("v_min3_f32 %0, %1, %2, %3" : "=v"(r) : "v"(a), "v"(b), "v"(c)); return r; }
DI void hedgehog2(const f32x4 (&a)[2], const f32x4 (&b)[2], int g, u32x4 (&f0)[2], u32x2 (&f1)[2]) {
  const float NEG = -__builtin_inff(), POS = __builtin_inff();
  constexpr float L2E = 1.4426950408889634f;
  float mx[2], mn[2];
#pragma unroll
  for (int i = 0; i < 2; ++i) {
    const float bm = max3f(max3f(b[i][0], b[i][1], b[i][2]), b[i][3], NEG);
    mx[i] = max3f(max3f(a[i][0], a[i][1], a[i][2]), a[i][3], g == 0 ? bm : NEG); mn[i] = max3f(-min3f(min3f(a[i][0], a[i][1], a[i][2]), a[i][3], POS), g == 1 ? bm : NEG, NEG); }
  asm volatile("s_nop 1" : "+v"(mx[0]), "+v"(mx[1]), "+v"(mn[0]), "+v"(mn[1]));
  xrow4<true>(mx[0], mx[1], mn[0], mn[1]);
  float sp[2], sm[2]; f32x4 p0[2], m0[2], e[2];
#pragma unroll
  for (int i = 0; i < 2; ++i) { mx[i] *= L2E; mn[i] *= L2E; const float rb = g == 0 ? mx[i] : (g == 1 ? mn[i] : POS);
#pragma unroll
    for (int r = 0; r < 4; ++r) { p0[i][r] = __builtin_amdgcn_exp2f(fmaf(a[i][r], L2E, -mx[i])); m0[i][r] = __builtin_amdgcn_exp2f(fmaf(a[i][r], -L2E, -mn[i])); e[i][r] = __builtin_amdgcn_exp2f(fmaf(b[i][r], L2E, -rb)); }
    const float sb = (e[i][0] + e[i][1]) + (e[i][2] + e[i][3]);
    sp[i] = ((p0[i][0] + p0[i][1]) + (p0[i][2] + p0[i][3])) + (g == 0 ? sb : 0.f); sm[i] = ((m0[i][0] + m0[i][1]) + (m0[i][2] + m0[i][3])) + (g == 1 ? sb : 0.f); }
  xrow4<false>(sp[0], sp[1], sm[0], sm[1]);
#pragma unroll
  for (int i = 0; i < 2; ++i) { const float ip = __builtin_amdgcn_rcpf(sp[i]), im = __builtin_amdgcn_rcpf(sm[i]);
    const f32x4 p = p0[i] * ip, m = m0[i] * im, x = e[i] * (g == 0 ? ip : im);
    f0[i] = (u32x4){cvtpk(p[0], p[1]), cvtpk(p[2], p[3]), cvtpk(m[0], m[1]), cvtpk(m[2], m[3])}; f1[i] = (u32x2){cvtpk(x[0], x[1]), cvtpk(x[2], x[3])}; }
}
DI u32x4 pair_swap(unsigned x0, unsigned x1, unsigned y0, unsigned y1) {
  auto a = __builtin_amdgcn_permlane16_swap(x0, y0, false, false);
  auto b = __builtin_amdgcn_permlane16_swap(x1, y1, false, false);
  return (u32x4){a[0], b[0], a[1], b[1]};
}

namespace proj {
constexpr int LDS_X = 0, LDS_W = 65536, LDS_T = 131072, LDS_BYTES = 163840;
constexpr float QSCALE = 0.08838834764831845f * 1.4426950408889634f;

struct Src {
  rsrc_t X, W; unsigned vX[4], vW[2]; unsigned sX, sW, wstep, rowX8;
  unsigned rowW2;
};
struct Frag { bf16x8 x[8]; s16x4 wl[2][4], wh[2][4]; };
#define WF(ks, n) __builtin_shufflevector(fr.wl[ks][n], fr.wh[ks][n], 0, 1, 2, 3, 4, 5, 6, 7)
struct Adr { const LAS char* x[2][2]; const LAS char* w[4]; };

enum { U_Q = 0, U_K = 1, U_V = 2, U_B = 3 };
struct Unit { int kind, pm, pn; };

struct Ptrs { const u16 *Ax, *ckv, *Bx, *wq, *wuk, *wuv, *wqkv; };
DI int fresh_lane() { int l; asm volatile("v_mbcnt_lo_u32_b32 %0, -1, 0\n\tv_mbcnt_hi_u32_b32 %0, -1, %0" : "=v"(l)); return l; }
DI void setup_src(Src& s, const Unit& u, const Ptrs& P, int wid) {
  const int lane = fresh_lane();
  const u16 *Ax = P.Ax, *ckv = P.ckv, *Bx = P.Bx, *wq = P.wq, *wuk = P.wuk, *wuv = P.wuv, *wqkv = P.wqkv;
  const int ldx = (u.kind == U_K || u.kind == U_V) ? 512 : 640;
  const u16* X = u.kind == U_Q ? Ax : (u.kind == U_B ? Bx : ckv);
  s.X = mk_rsrc(X, 2048u * (unsigned)ldx * 2u);
  const int r = lane >> 3, f0 = ((r >> 1) & 1) | (((r >> 2) & 1) << 1);
#pragma unroll
  for (int i = 0; i < 4; ++i) s.vX[i] = (unsigned)(r * ldx * 2) + (unsigned)((((lane & 7) ^ f0 ^ (((i >> 1) & 1) << 1) ^ ((i & 1) << 2)) & 7) * 16);
  s.rowX8 = (unsigned)(8 * ldx * 2);
  s.sX = (unsigned)((u.pm * 256 + wid * 32) * ldx * 2);
  if (u.kind == U_B) {
    s.W = mk_rsrc(wqkv, 640u * 1920u * 2u);
#pragma unroll
    for (int p = 0; p < 2; ++p) { const int sg = ((2 * p + (lane >> 5)) & 3) | ((wid & 1) << 2); const int ch = (lane & 31) ^ (sg << 1);
      const int col = ch < 10 ? u.pn * 160 + ch * 16 : (ch < 20 ? 1280 + u.pn * 160 + (ch - 10) * 16 : (ch < 30 ? 2560 + u.pn * 160 + (ch - 20) * 16 : u.pn * 160));
      s.vW[p] = (unsigned)((lane >> 5) * 3840 + col); }
    s.rowW2 = 2u * 3840u; s.wstep = 64u * 3840u; s.sW = (unsigned)(wid * 8 * 3840);
  } else {
    const u16* W = u.kind == U_Q ? wq : (u.kind == U_K ? wuk : wuv);
    s.W = mk_rsrc(W, (u.kind == U_Q ? 640u : 512u) * 4096u * 2u);
#pragma unroll
    for (int p = 0; p < 2; ++p) { const int sg = ((2 * p + (lane >> 5)) & 3) | ((wid & 1) << 2); s.vW[p] = (unsigned)((lane >> 5) * 8192 + (((lane & 31) ^ (sg << 1)) * 16)); }
    s.rowW2 = 2u * 8192u; s.wstep = 64u * 8192u; s.sW = (unsigned)(wid * 8 * 8192 + u.pn * 512);
  }
}
template <int J> DI void dma_piece(const Src& s, unsigned ldsw, int slot) {
  if constexpr (J < 4) dma16(ldsw + LDS_X + slot * 32768 + J * 1024, s.vX[J], s.X, s.sX + J * s.rowX8);
  else dma16(ldsw + LDS_W + slot * 32768 + (J - 4) * 1024, s.vW[(J - 4) & 1], s.W, s.sW + (J - 4) * s.rowW2);
}
DI void src_next(Src& s) { s.sX += 128u; s.sW += s.wstep; }

DI void setup_adr(Adr& a, const LAS char* lds, bool kappa, int wr, int wc) {
  const int lane = fresh_lane();
  const int c = lane & 15, g = lane >> 4;
#pragma unroll
  for (int par = 0; par < 2; ++par) {
    const int row = kappa ? 8 * (c >> 2) + 4 * par + (c & 3) : 16 * par + c;
    const int f = ((row >> 1) & 1) | ((((row >> 2) ^ (row >> 4)) & 1) << 1) | (((row >> 3) & 1) << 2);
#pragma unroll
    for (int ks = 0; ks < 2; ++ks) a.x[par][ks] = lds + LDS_X + wr * 16384 + row * 128 + (((4 * ks + g) ^ f) * 16);
  }
  const int q = (lane & 15) >> 2, p = lane & 3, sg = q | ((g & 1) << 2);
#pragma unroll
  for (int n = 0; n < 4; ++n) a.w[n] = lds + LDS_W + (8 * g + q) * 512 + (((4 * wc + n) ^ sg) * 32) + 8 * p;
}
struct Pf { const char* p; unsigned step; int left; };
DI void setup_pf(Pf& f, int gw, int first, const Ptrs& P, const u16* wo, const u16* Bwo, bool on) {
  const int lane = fresh_lane();
  const int L = gw * 10 + lane;
  const char* p = (const char*)P.Ax; unsigned step = 0; int left = 0;
  if (lane < 10) {
    if (L < 2048) { p = (const char*)P.Ax + L * 1280; step = 128; left = 10; }
    else if (L < 4096) { p = (const char*)P.Bx + (L - 2048) * 1280; step = 128; left = 10; }
    else if (L < 6144) { p = (const char*)P.ckv + (L - 4096) * 1024; step = 128; left = 8; }
    else if (L < 10240) { p = (const char*)P.wq + ((L - 6144) >> 6) * 8192 + ((L - 6144) & 63) * 128; step = 64 * 8192; left = 10; }
    else if (L < 14336) { p = (const char*)P.wuk + ((L - 10240) >> 6) * 8192 + ((L - 10240) & 63) * 128; step = 64 * 8192; left = 8; }
    else if (L < 18432) { p = (const char*)P.wuv + ((L - 14336) >> 6) * 8192 + ((L - 14336) & 63) * 128; step = 64 * 8192; left = 8; }
    else if (L < 20352) { const int q = L - 18432, r = q / 30; p = (const char*)P.wqkv + r * 3840 + (q - r * 30) * 128; step = 64 * 3840; left = 10; }
    p += (size_t)first * step; left -= first;
  } else if (lane < 13) { const int q = gw * 3 + (lane - 10); p = (const char*)wo + (size_t)q * 128; step = 6144 * 128; left = q < 4096 ? 7 : 6; }
  else if (lane == 13) { p = (const char*)Bwo + (size_t)gw * 128; step = 2048 * 128; left = gw < 256 ? 4 : 3; }
  if (!on) left = 0;
  f.p = p; f.step = step; f.left = left;
}
DI void tlb_touch(int x, int wv  , const Ptrs& P, const u16* Qf, const u16* Kf, const u16* Vf, unsigned ldsa) {
  const int lane = fresh_lane(), q = wv * 22 + lane;
  const char* p = (const char*)P.Ax;
  if (lane < 22) {
    if (q < 640) p = (const char*)P.Ax + q * 4096;
    else if (q < 1280) p = (const char*)P.Bx + (q - 640) * 4096;
    else if (q < 1792) p = (const char*)P.ckv + (q - 1280) * 4096;
    else if (q < 2432) p = (const char*)P.wq + (q - 1792) * 8192 + x * 1024;
    else if (q < 2944) p = (const char*)P.wuk + (q - 2432) * 8192 + x * 1024;
    else if (q < 3456) p = (const char*)P.wuv + (q - 2944) * 8192 + x * 1024;
    else if (q < 4096) p = (const char*)P.wqkv + (q - 3456) * 3840 + x * 160;
    else if (q < 4608) p = (const char*)Qf + (size_t)x * 2097152 + (q - 4096) * 4096;
    else if (q < 5120) p = (const char*)Kf + (size_t)x * 2097152 + (q - 4608) * 4096;
    else if (q < 5632) p = (const char*)Vf + (size_t)x * 2097152 + (q - 5120) * 4096;
  }
  touch_glb(ldsa, p);
}
DI void pf_touch(Pf& f, const char* dummy, unsigned ldsa) { touch_glb(ldsa, f.left > 0 ? f.p : dummy); f.p += f.step; f.left -= 1; }
#define RDX(SL, f) (fr.x[(f) & 7] = lds_rd128(ad.x[(f) & 1][((f) >> 3) & 1] + (SL) * 32768 + (((f) & 7) >> 1) * 4096))
#define RDW(SL, ks, n, h) do { if (h) fr.wh[ks][n] = lds_rdtr(ad.w[n] + (SL) * 32768 + (ks) * 16384 + 2048); else fr.wl[ks][n] = lds_rdtr(ad.w[n] + (SL) * 32768 + (ks) * 16384); } while (0)

#define ST_AUX 16
#define ST_MID 16
struct Epi { rsrc_t r; unsigned voff; float scale; bool on, mid; };
DI void epi_store(const u32x4& w, const Epi& e, unsigned voff) { if (e.mid) st128<ST_MID>(w, e.r, voff); else st128<ST_AUX>(w, e.r, voff); }
DI void setup_epi(Epi& e, const Unit& u, u16* Qf, u16* Kf, u16* Vf, int wr, int wc) {
  const int lane = fresh_lane(), g = lane >> 4;
  e.on = u.kind != U_B; e.scale = u.kind == U_Q ? QSCALE : 1.f;
  e.r = mk_rsrc(u.kind == U_Q ? Qf : (u.kind == U_K ? Kf : Vf), 2048u * 4096u * 2u);
  if (u.kind == U_V) e.voff = (unsigned)(((u.pn * 2 + (wc >> 1)) * 64 + u.pm * 8 + wr * 4) * 8 + (wc & 1) * 4) * 1024u + (unsigned)lane * 16u;
  else e.voff = (unsigned)(((u.pn * 2 + (wc >> 1)) * 128 + u.pm * 16 + wr * 8) * 4 + (wc & 1) * 2) * 1024u + (unsigned)((lane & 15) + 16 * (((g & 1) << 1) | (g >> 1))) * 16u;
}
template <int M> DI void epi_qk(const f32x4 (&acc)[8][4], const Epi& e) {
#pragma unroll
  for (int t = 0; t < 2; ++t) { const f32x4 a = acc[M][2 * t] * e.scale, b = acc[M][2 * t + 1] * e.scale;
    epi_store(pair_swap(cvtpk(a[0], a[1]), cvtpk(a[2], a[3]), cvtpk(b[0], b[1]), cvtpk(b[2], b[3])), e, e.voff + M * 4096 + t * 1024); }
}
template <int MM, int H> DI void epi_v(const f32x4 (&acc)[8][4], const Epi& e) {
#pragma unroll
  for (int n = 2 * H; n < 2 * H + 2; ++n) { const f32x4 a = acc[2 * MM][n], b = acc[2 * MM + 1][n];
    epi_store((u32x4){cvtpk(a[0], a[1]), cvtpk(a[2], a[3]), cvtpk(b[0], b[1]), cvtpk(b[2], b[3])}, e, e.voff + MM * 8192 + n * 1024); }
}

DI void xwait(int mode) {
  if (mode == 0) asm volatile("s_waitcnt vmcnt(0) lgkmcnt(0)\n\ts_barrier" ::: "memory");
  else if (mode == 1) asm volatile("s_waitcnt vmcnt(1) lgkmcnt(0)\n\ts_barrier" ::: "memory");
  else if (mode == 2) asm volatile("s_waitcnt vmcnt(2) lgkmcnt(0)\n\ts_barrier" ::: "memory");
  else asm volatile("s_waitcnt vmcnt(16) lgkmcnt(0)\n\ts_barrier" ::: "memory");
}
template <int SL, bool FIRST, bool SWAP, bool LAST = false>
DI void ktile(f32x4 (&acc)[8][4], Frag& fr, const Adr& ad, Src& src, Pf& pf, const Unit& nu, const Ptrs& P, int wid, bool switch_src, bool do_dma, int wmode, bool touch, unsigned ldsw, const Epi& ep) {
#define MM(f, n) do { \
    if (FIRST && (f) < 8) { if (SWAP) mfma_zero(acc[(f) & 7][n], fr.x[(f) & 7], WF(0, n)); else mfma_zero(acc[(f) & 7][n], WF(0, n), fr.x[(f) & 7]); } \
    else { if (SWAP) mfma_acc(acc[(f) & 7][n], fr.x[(f) & 7], WF((f) >> 3, n)); else mfma_acc(acc[(f) & 7][n], WF((f) >> 3, n), fr.x[(f) & 7]); } SBAR(); } while (0)
#define GRP(f) do { \
    if ((f) == 9) { xwait(wmode); SBAR(); if (switch_src) setup_src(src, nu, P, wid); } \
    MM(f, 0); \
    if ((f) < 4) { RDW(SL, 1, (f) & 3, 0); } else if (!LAST && (f) >= 9 && (f) < 13) { RDW(SL ^ 1, 0, ((f) - 9) & 3, 0); } \
    SBAR(); \
    MM(f, 1); \
    if ((f) < 4) { RDW(SL, 1, (f) & 3, 1); } else if (!LAST && (f) >= 9 && (f) < 13) { RDW(SL ^ 1, 0, ((f) - 9) & 3, 1); } \
    SBAR(); \
    MM(f, 2); \
    if (do_dma && !LAST) { if ((f) == 9) dma_piece<0>(src, ldsw, SL); if ((f) == 10) dma_piece<4>(src, ldsw, SL); if ((f) == 11) dma_piece<1>(src, ldsw, SL); if ((f) == 12) dma_piece<5>(src, ldsw, SL); \
                  if ((f) == 13) dma_piece<2>(src, ldsw, SL); if ((f) == 14) dma_piece<6>(src, ldsw, SL); if ((f) == 15) { dma_piece<3>(src, ldsw, SL); } } \
    if (do_dma && LAST) {   \
                  if ((f) == 9) { dma_piece<0>(src, ldsw, SL); dma_piece<4>(src, ldsw, SL); dma_piece<1>(src, ldsw, SL); dma_piece<5>(src, ldsw, SL); } \
                  if ((f) == 10) { dma_piece<2>(src, ldsw, SL); dma_piece<6>(src, ldsw, SL); dma_piece<3>(src, ldsw, SL); dma_piece<7>(src, ldsw, SL); src_next(src); } } \
    SBAR(); \
    MM(f, 3); \
    if ((f) == 15) { if (do_dma && !LAST) { dma_piece<7>(src, ldsw, SL); src_next(src); } if (touch) pf_touch(pf, (const char*)P.Ax, ldsw - (unsigned)wid * 4096u + LDS_T + (unsigned)wid * 256u); } \
    if ((f) < 8) RDX(SL, (f) + 8); else if (!LAST) { if ((f) == 9) { RDX(SL ^ 1, 0); SBAR(); RDX(SL ^ 1, 1); } else if ((f) > 9) RDX(SL ^ 1, (f) - 8); } \
    SBAR(); \
    if (LAST && (f) >= 10 && ep.on) {   \
      if (!SWAP) epi_qk<((f) >= 10 ? (f) - 10 : 0)>(acc, ep); else if ((f) >= 11) epi_v<((f) >= 11 ? ((f) - 11) >> 1 : 0), ((f) - 11) & 1>(acc, ep); SBAR(); } \
    } while (0)
  GRP(0); GRP(1); GRP(2); GRP(3); GRP(4); GRP(5); GRP(6); GRP(7); GRP(8); GRP(9); GRP(10); GRP(11); GRP(12); GRP(13); GRP(14); GRP(15);
#undef GRP
#undef MM
}

template <bool SWAP>
DI void run_unit(f32x4 (&acc)[8][4], const Adr& ad, Src& src, Pf& pf, bool warm, tag_t* flags, tag_t tag, const Unit& nu, const Ptrs& P, int wid, bool has_next, int nt, unsigned ldsw, const Unit& cu, u16* Qf, u16* Kf, u16* Vf) {
  Epi ep; ep.on = false; ep.mid = false; ep.scale = 1.f; ep.voff = 0; ep.r = src.X;
  Frag fr;
  if (SWAP) asm volatile("; unit: tokens on rows" ::: "memory"); else asm volatile("; unit: weights on rows" ::: "memory");
  RDW(0, 0, 0, 0); RDW(0, 0, 0, 1); RDX(0, 0); RDW(0, 0, 1, 0); RDW(0, 0, 1, 1); RDW(0, 0, 2, 0); RDW(0, 0, 2, 1); RDW(0, 0, 3, 0); RDW(0, 0, 3, 1);
  RDX(0, 1); RDX(0, 2); RDX(0, 3); RDX(0, 4); RDX(0, 5); RDX(0, 6); RDX(0, 7);
  SBAR();
  ktile<0, true, SWAP>(acc, fr, ad, src, pf, nu, P, wid, false, true, warm ? 2 : 3, warm, ldsw, ep);
  ktile<1, false, SWAP>(acc, fr, ad, src, pf, nu, P, wid, false, true, warm ? 1 : 0, warm, ldsw, ep);
  if (!warm && wid == 0 && fresh_lane() == 0) flag_set(flags, FL_U0 + (int)blockIdx.x, tag);
  for (int t = 2; t < nt - 2; t += 2) {
    ktile<0, false, SWAP>(acc, fr, ad, src, pf, nu, P, wid, false, true, warm && t <= 6 ? 1 : 0, warm && t < 6, ldsw, ep);
    ktile<1, false, SWAP>(acc, fr, ad, src, pf, nu, P, wid, false, true, warm && t + 1 <= 6 ? 1 : 0, warm && t + 1 < 6, ldsw, ep);
  }
  ktile<0, false, SWAP>(acc, fr, ad, src, pf, nu, P, wid, has_next, has_next, warm && nt - 2 <= 6 ? 1 : 0, false, ldsw, ep);
  setup_epi(ep, cu, Qf, Kf, Vf, wid >> 2, wid & 3); ep.mid = has_next;
  ktile<1, false, SWAP, true>(acc, fr, ad, src, pf, nu, P, wid, false, has_next, 0, false, ldsw, ep);
  MFMA_SETTLE();
  if (ep.on) {
    if (!SWAP) { epi_qk<6>(acc, ep); epi_qk<7>(acc, ep); } else { epi_v<2, 1>(acc, ep); epi_v<3, 0>(acc, ep); epi_v<3, 1>(acc, ep); }
  }
}

DI void relayout_issue(const u16* __restrict__ wo, const u16* __restrict__ Bwo, const LAS char* lds, int wid, int gw) {
  const int lane = fresh_lane();
  const unsigned ldsw = (unsigned)(uintptr_t)lds + (unsigned)wid * 16384u;
  const unsigned voff = (unsigned)((lane >> 3) * 1280 + (lane & 7) * 16);
#pragma unroll
  for (int e = 0; e < 3; ++e) { const int x = gw + e * 512; const int ks = x / 10, ng = x - ks * 10; const bool isA = ks < 128;
    const rsrc_t r = mk_rsrc(isA ? wo : Bwo, x < 1480 ? (isA ? 4096u * 640u * 2u : 640u * 640u * 2u) : 0u);
    const unsigned so = (unsigned)((isA ? ks : ks - 128) * 32 * 1280 + ng * 128);
#pragma unroll
    for (int n = 0; n < 4; ++n) dma16(ldsw + (unsigned)(e * 4 + n) * 1024u, voff, r, so + n * 8u * 1280u); }
}
DI void relayout_finish(u16* __restrict__ WfA, u16* __restrict__ WfB, const LAS char* lds, int wid, int gw) {
  const int lane = fresh_lane(), g = lane >> 4, q4 = (lane & 15) >> 2, p4 = lane & 3;
  const LAS char* rd = lds + wid * 16384 + (8 * g + q4) * 128 + 8 * p4;
  const rsrc_t rA = mk_rsrc(WfA, 4096u * 640u * 2u), rB = mk_rsrc(WfB, 640u * 640u * 2u);
  asm volatile("s_waitcnt vmcnt(0)" ::: "memory");
#pragma unroll
  for (int e = 0; e < 3; ++e) { const int x = gw + e * 512; const int ks = x / 10, ng = x - ks * 10;
#pragma unroll
    for (int n = 0; n < 4; ++n) { const LAS char* a = rd + e * 4096 + n * 32;
      const bf16x8 w = __builtin_shufflevector(lds_rdtr(a), lds_rdtr(a + 4 * 128), 0, 1, 2, 3, 4, 5, 6, 7);
      const unsigned off = (unsigned)(((ks < 128 ? ks : ks - 128) * 40 + ng * 4 + n) * 64 + lane) * 16u;
      st128<ST_AUX>(__builtin_bit_cast(u32x4, w), ks < 128 ? rA : rB, x < 1480 ? off : 0x80000000u); } }
}

constexpr int T_PITCH = 528, PH_OFF = 256 * T_PITCH, PH_PITCH = 96, PH_WAVE = 32 * PH_PITCH;
static_assert(PH_OFF + 8 * PH_WAVE + 6 * 1024 <= LDS_BYTES + 6 * 1024 && PH_OFF + 8 * PH_WAVE <= LDS_BYTES, "LDS map");
#define BSTAMP(i)
constexpr int WPHI_OFF = 159744;
DI void epi_b(const f32x4 (&acc)[8][4], const Unit& u, const LAS char* lds_c, u16* __restrict__ PhiQ, float* __restrict__ Part, const u16* __restrict__ wo, const u16* __restrict__ Bwo, int gw, int wid, unsigned long long* bts) {
  LAS char* lds = (LAS char*)lds_c;
  const int lane = fresh_lane(), c = lane & 15, g = lane >> 4, gp = ((g & 1) << 1) | (g >> 1), wr = wid >> 2, wc = wid & 3;
  const int hh = wid & 3, half = wid >> 2, head = u.pn * 4 + hh;
  asm volatile("s_waitcnt vmcnt(0) lgkmcnt(0)\n\ts_barrier" ::: "memory");
#pragma unroll
  for (int m = 0; m < 8; ++m)
#pragma unroll
    for (int t = 0; t < 2; ++t) { const f32x4 a = acc[m][2 * t], b = acc[m][2 * t + 1];
      *(LAS u32x4*)(lds + (wr * 128 + m * 16 + c) * T_PITCH + (wc * 64 + t * 32 + gp * 8) * 2) = pair_swap(cvtpk(a[0], a[1]), cvtpk(a[2], a[3]), cvtpk(b[0], b[1]), cvtpk(b[2], b[3])); }
  SBAR();
  bf16x8 w0, w1;
  { const LAS u16* wp = (const LAS u16*)(lds + WPHI_OFF) + hh * 400;
#pragma unroll
    for (int j = 0; j < 8; ++j) { const int d = 8 * g + j, dd = d < 20 ? d : 0; const u16 a = wp[dd * 20 + c], b = wp[dd * 20 + 16 + (c & 3)];
      w0[j] = d < 20 ? (short)a : (short)0; w1[j] = (d < 20 && c < 8) ? (short)(c < 4 ? b : (u16)(b ^ 0x8000u)) : (short)0; } }
  asm volatile("s_waitcnt lgkmcnt(0)\n\ts_barrier" ::: "memory");
  BSTAMP(0);
  LAS char* ph = lds + PH_OFF + wid * PH_WAVE;
  const rsrc_t rq = mk_rsrc(PhiQ, 32u * 128u * 1024u + 32u * 2048u);
  if (u.pm == 0 && half == 0) { st128<ST_AUX>(__builtin_bit_cast(u32x4, w0), rq, 32u * 128u * 1024u + (unsigned)head * 2048u + (unsigned)lane * 16u); st128<ST_AUX>(__builtin_bit_cast(u32x4, w1), rq, 32u * 128u * 1024u + (unsigned)head * 2048u + 1024u + (unsigned)lane * 16u); }
  f32x4 kv[3][2];
#pragma unroll
  for (int i = 0; i < 3; ++i) { kv[i][0] = (f32x4){}; kv[i][1] = (f32x4){}; }
  const int q4 = c >> 2, p4 = c & 3;
  const unsigned mlo = g == 3 ? 0u : 0xffffffffu, mhi = g >= 2 ? 0u : 0xffffffffu;
  for (int step = 0; step < 4; ++step) {
    const int r0 = half * 128 + step * 32;
    f32x4 a[2], b[2];
#pragma unroll
    for (int i = 0; i < 2; ++i) { const LAS char* xr = lds + (r0 + i * 16 + c) * T_PITCH + (hh * 20 + 8 * g) * 2;
      u32x2 lo = *(const LAS u32x2*)(xr + 160), hi = *(const LAS u32x2*)(xr + 168); lo &= mlo; hi &= mhi;
      const bf16x8 x = __builtin_bit_cast(bf16x8, (u32x4){lo[0], lo[1], hi[0], hi[1]});
      a[i] = MFMA16(w0, x, ((f32x4){})); b[i] = MFMA16(w1, x, ((f32x4){}));
      u32x2 ql = *(const LAS u32x2*)xr, qh = *(const LAS u32x2*)(xr + 8); ql &= mlo; qh &= mhi;
      st128<ST_AUX>((u32x4){ql[0], ql[1], qh[0], qh[1]}, rq, (unsigned)(head * 128 + u.pm * 16 + (r0 >> 4) + i) * 1024u + (unsigned)lane * 16u); }
    asm volatile("s_nop 7\n\ts_nop 1" : "+v"(a[0]), "+v"(a[1]), "+v"(b[0]), "+v"(b[1]));
    u32x4 f0[2]; u32x2 f1[2];
    hedgehog2(a, b, g, f0, f1);
#pragma unroll
    for (int i = 0; i < 2; ++i) { LAS char* pr = ph + (i * 16 + c) * PH_PITCH + 8 * g;
      *(LAS u32x2*)(pr) = (u32x2){f0[i][0], f0[i][1]}; *(LAS u32x2*)(pr + 32) = (u32x2){f0[i][2], f0[i][3]}; *(LAS u32x2*)(pr + 64) = f1[i]; }
    asm volatile("s_waitcnt lgkmcnt(0)" ::: "memory");
    bf16x8 vb[2];
#pragma unroll
    for (int db = 0; db < 2; ++db) { const LAS char* av = lds + (r0 + 8 * g + q4) * T_PITCH + (160 + hh * 20 + 16 * db + 4 * p4) * 2;
      vb[db] = __builtin_shufflevector(lds_rdtr(av), lds_rdtr(av + 4 * T_PITCH), 0, 1, 2, 3, 4, 5, 6, 7); }
    if (c == 4) vb[1] = (bf16x8){0x3F80, 0x3F80, 0x3F80, 0x3F80, 0x3F80, 0x3F80, 0x3F80, 0x3F80};
#pragma unroll
    for (int ab = 0; ab < 3; ++ab) { const LAS char* ap = ph + (8 * g + q4) * PH_PITCH + (16 * ab + 4 * p4) * 2;
      const bf16x8 pa = __builtin_shufflevector(lds_rdtr(ap), lds_rdtr(ap + 4 * PH_PITCH), 0, 1, 2, 3, 4, 5, 6, 7);
      kv[ab][0] = MFMA16(pa, vb[0], kv[ab][0]); kv[ab][1] = MFMA16(pa, vb[1], kv[ab][1]); }
    asm volatile("s_waitcnt lgkmcnt(0)" ::: "memory");
  }
  asm volatile("s_nop 7\n\ts_nop 1" : "+v"(kv[0][0]), "+v"(kv[0][1]), "+v"(kv[1][0]), "+v"(kv[1][1]), "+v"(kv[2][0]), "+v"(kv[2][1]));
  SBAR();
  BSTAMP(1);
  asm volatile("s_barrier" ::: "memory");
  relayout_issue(wo, Bwo, lds_c, wid, gw);
  LAS char* ex = lds + PH_OFF + hh * 6144 + lane * 16;
  if (half == 1) {
#pragma unroll
    for (int i = 0; i < 6; ++i) *(LAS f32x4*)(ex + i * 1024) = kv[i >> 1][i & 1]; }
  asm volatile("s_waitcnt lgkmcnt(0)\n\ts_barrier" ::: "memory");
  if (half == 0) {
#pragma unroll
    for (int i = 0; i < 6; ++i) st128<ST_AUX>(__builtin_bit_cast(u32x4, kv[i >> 1][i & 1] + *(const LAS f32x4*)(ex + i * 1024)), mk_rsrc(Part, 8u * 32u * 6144u), (unsigned)(((u.pm * 32 + head) * 6 + i) * 64 + lane) * 16u); }
}
DI void phase1(const u16* __restrict__ Ax, const u16* __restrict__ ckv, const u16* __restrict__ wq, const u16* __restrict__ wuk, const u16* __restrict__ wuv,
               const u16* __restrict__ Bx, const u16* __restrict__ wqkv, u16* __restrict__ Qf, u16* __restrict__ Kf, u16* __restrict__ Vf,
               const u16* __restrict__ wphi, u16* __restrict__ PhiQ, float* __restrict__ Part,
               const u16* __restrict__ wo, const u16* __restrict__ Bwo, u16* __restrict__ WfA, u16* __restrict__ WfB, tag_t* __restrict__ flags, tag_t tag, unsigned long long* dbg) {
  extern __shared__ __attribute__((aligned(16))) char smem[];
  const LAS char* lds = (const LAS char*)smem;
  const int tid = threadIdx.x, lane = tid & 63, wid = (int)rfl(tid >> 6), wr = wid >> 2, wc = wid & 3;
  const unsigned ldsw = (unsigned)(uintptr_t)lds + (unsigned)wid * 4096u;
  const int x = blockIdx.x & 7, j = blockIdx.x >> 3;
  auto kv_unit = [&](int k) { Unit u; u.kind = (k & 2) ? U_V : U_K; u.pn = 2 * x + (k & 1); u.pm = k >> 2; return u; };
  Unit u0, u1; bool two = true;
  if (j < 16) { u0.kind = U_Q; u0.pn = 2 * x + (j & 1); u0.pm = j >> 1; u1 = kv_unit(16 + j); }
  else if (j < 24) { u0 = kv_unit(j - 16); u1 = kv_unit(8 + j - 16); }
  else { u0.kind = U_B; u0.pn = x; u0.pm = j - 24; u1 = u0; two = false; }
  const Ptrs P = {Ax, ckv, Bx, wq, wuk, wuv, wqkv};
  Src src;
  setup_src(src, u0, P, wid);
  dma_piece<0>(src, ldsw, 0); dma_piece<4>(src, ldsw, 0); dma_piece<1>(src, ldsw, 0); dma_piece<5>(src, ldsw, 0); dma_piece<2>(src, ldsw, 0); dma_piece<6>(src, ldsw, 0); dma_piece<3>(src, ldsw, 0); dma_piece<7>(src, ldsw, 0); src_next(src);
  dma_piece<0>(src, ldsw, 1); dma_piece<4>(src, ldsw, 1); dma_piece<1>(src, ldsw, 1); dma_piece<5>(src, ldsw, 1); dma_piece<2>(src, ldsw, 1); dma_piece<6>(src, ldsw, 1); dma_piece<3>(src, ldsw, 1); dma_piece<7>(src, ldsw, 1); src_next(src);
  Pf pf; setup_pf(pf, (int)blockIdx.x * 8 + wid, 2, P, wo, Bwo, true);
  f32x4 acc[8][4]; Adr ad;
  if (u0.kind == U_B && wid < 4) dma16((unsigned)(uintptr_t)lds + WPHI_OFF + (unsigned)wid * 1024u, (unsigned)lane * 16u, mk_rsrc(wphi + u0.pn * 1600, 3200u), (unsigned)wid * 1024u);
  Unit u = u0;
  asm volatile("s_waitcnt vmcnt(8)\n\ts_barrier" ::: "memory");
  SBAR();
  pf_touch(pf, (const char*)Ax, (unsigned)(uintptr_t)lds + LDS_T + (unsigned)wid * 256u); pf_touch(pf, (const char*)Ax, (unsigned)(uintptr_t)lds + LDS_T + (unsigned)wid * 256u);
  SBAR();
  for (int ui = 0; ui < 2; ++ui) {
    const bool has_next = two && ui == 0;
    const int nt = (u.kind == U_K || u.kind == U_V) ? 8 : 10;
    setup_adr(ad, lds, u.kind == U_K || u.kind == U_V, wr, wc);
    if (u.kind == U_V) run_unit<true>(acc, ad, src, pf, ui == 0, flags, tag, u1, P, wid, has_next, nt, ldsw, u, Qf, Kf, Vf);
    else run_unit<false>(acc, ad, src, pf, ui == 0, flags, tag, u1, P, wid, has_next, nt, ldsw, u, Qf, Kf, Vf);
    if (u.kind == U_B) {
      unsigned long long bts[4] = {};
      epi_b(acc, u, lds, PhiQ, Part, wo, Bwo, (x * 8 + u.pm) * 8 + wid, wid, bts);
      relayout_finish(WfA, WfB, lds, wid, (x * 8 + u.pm) * 8 + wid);
      BSTAMP(2);
    }
    if (!has_next) break;
    u = u1;
  }
  asm volatile("s_waitcnt vmcnt(0) lgkmcnt(0)\n\ts_barrier" ::: "memory");
  if (tid == 0) flag_set(flags, FL_P1 + (int)blockIdx.x, tag);
}
}

#define MFMASUM 1
#define CF_AUX 16
namespace attn {
constexpr int LDS_K = 0, LDS_V = 65536, LDS_X = 131072, LDS_BYTES = 163840;
constexpr float L_LIMIT = 1.2676506e30f;

#define ITEMS 0x43726510u, 0xbcaf89deu, 0x4016752353276410ull
template <unsigned A, unsigned B, unsigned long long C> struct ItemTab { static constexpr unsigned light = A, heavy = B; static constexpr unsigned long long owner = C; };
typedef ItemTab<ITEMS> Items;
constexpr unsigned ITEM_LIGHT = Items::light, ITEM_HEAVY = Items::heavy;
constexpr unsigned long long BLOCK_ITEM = Items::owner;
DI void item_of(int j, int& hl, int& item) { if (j < 16) { hl = j >> 2; item = j & 3; } else if (j < 24) { hl = (j - 16) >> 1; item = 4 + (j & 1); } else { hl = (j - 24) >> 1; item = 6 + (j & 1); } }
DI int wg_of(int head, int item) { const int hl = head & 3; const int j = item < 4 ? hl * 4 + item : (item < 6 ? 16 + hl * 2 + (item - 4) : 24 + hl * 2 + (item - 6)); return (head >> 2) + 8 * j; }

struct Til { f32x4 s[8]; };
struct St {
  f32x4 o[16];
  bf16x8 q[8];
  f32x4 negm[2];
  bf16x8 fr[8];
  u32x4 pw[4];
  f32x2 l[2];
  f32x4 ls[2]; bf16x8 ones;
  float m[2];
};

DI void cmask(Til& t, int thr) {
  const float NEG = -__builtin_inff();
#pragma unroll
  for (int kb = 0; kb < 4; ++kb)
#pragma unroll
    for (int qb = 0; qb < 2; ++qb)
#pragma unroll
      for (int r = 0; r < 4; ++r) if (32 * (kb >> 1) + 4 * (kb & 1) + r - 16 * qb > thr) t.s[2 * kb + qb][r] = NEG;
}

DI void safe_path(const u16* __restrict__ Qf, const u16* __restrict__ Kf, const u16* __restrict__ Vf, u16* __restrict__ Cf, int head, int blk, int rg, int grp, int lane) {
  const int g = lane >> 4, c = lane & 15;
  for (int qb = 0; qb < 2; ++qb) {
    const rsrc_t rq = mk_rsrc(Qf + (size_t)head * 262144, 524288u), rk = mk_rsrc(Kf + (size_t)head * 262144, 524288u), rv = mk_rsrc(Vf + (size_t)head * 262144, 524288u);
    bf16x8 q[4];
#pragma unroll
    for (int d = 0; d < 4; ++d) q[d] = ld128_dev(rq, (unsigned)((((blk * 4 + rg) * 8 + qb * 4 + d) * 64) + lane) * 16u);
    f32x4 o[4] = {};
    float m = -1e30f, l = 0.f;
    const int row = blk * 128 + rg * 32 + qb * 16 + c;
    const int nsteps = blk * 4 + rg + 1;
    for (int ks = 0; ks < nsteps; ++ks) {
      const unsigned ko = (unsigned)(((ks >> 1) * 16 + (ks & 1) * 8) * 64 + lane) * 16u;
      f32x4 s[2] = {};
      float mx = -__builtin_inff();
#pragma unroll
      for (int b = 0; b < 2; ++b) {
#pragma unroll
        for (int d = 0; d < 4; ++d) s[b] = MFMA16(ld128_dev(rk, ko + (unsigned)(b * 4 + d) * 1024u), q[d], s[b]);
#pragma unroll
        for (int r = 0; r < 4; ++r) { if (ks * 32 + 8 * g + 4 * b + r > row) s[b][r] = -__builtin_inff(); mx = fmaxf(mx, s[b][r]); }
      }
      mx = xrow_max(mx);
      const float mn = fmaxf(m, mx), al = __builtin_amdgcn_exp2f(m - mn); m = mn;
      float ps = 0.f;
#pragma unroll
      for (int b = 0; b < 2; ++b)
#pragma unroll
        for (int r = 0; r < 4; ++r) { s[b][r] = __builtin_amdgcn_exp2f(s[b][r] - mn); ps += s[b][r]; }
      l = l * al + ps;
      const u32x4 w = {cvtpk(s[0][0], s[0][1]), cvtpk(s[0][2], s[0][3]), cvtpk(s[1][0], s[1][1]), cvtpk(s[1][2], s[1][3])};
      const unsigned vo = ko + (unsigned)grp * 4096u;
#pragma unroll
      for (int db = 0; db < 4; ++db) { o[db] *= al; o[db] = MFMA16(ld128_dev(rv, vo + (unsigned)db * 1024u), __builtin_bit_cast(bf16x8, w), o[db]); }
    }
    const float il = 1.f / xrow_sum(l);
    const unsigned co = (unsigned)(((head * 4 + grp * 2) * 128 + blk * 8 + rg * 2 + qb) * 64 + (c + 16 * (((g & 1) << 1) | (g >> 1)))) * 16u;
#pragma unroll
    for (int t = 0; t < 2; ++t)
      st128<CF_AUX>(pair_swap(cvtpk(o[2 * t][0] * il, o[2 * t][1] * il), cvtpk(o[2 * t][2] * il, o[2 * t][3] * il), cvtpk(o[2 * t + 1][0] * il, o[2 * t + 1][1] * il), cvtpk(o[2 * t + 1][2] * il, o[2 * t + 1][3] * il)), mk_rsrc(Cf, 2048u * 4096u * 2u), co + (unsigned)t * 131072u);
  }
}

#define A_RDK(n) (st.fr[(n) & 7] = lds_rd128(kp + (n) * 1024))
#define A_RDV(n) (st.fr[(n) & 7] = lds_rd128(vp + (n) * 1024))
#define RDK2(a, b) do { A_RDK(a); SBAR(); A_RDK(b); } while (0)
#define RDV2(a, b) do { A_RDV(a); SBAR(); A_RDV(b); } while (0)
#define EXP2(x) __builtin_amdgcn_exp2f(x)

struct Dma { unsigned voff; rsrc_t srdK, srdV; unsigned kdst, vdst, ksoff, vsoff; bool k01, k23, v01, v23; };
template <int MODE, int J> DI void dma_piece(const Dma& d) {
  constexpr bool hi = (J & 2) != 0;
  const bool on = MODE == 2 ? true : (MODE == 1 ? !hi : (J < 4 ? (hi ? d.k23 : d.k01) : (hi ? d.v23 : d.v01)));
  if (on) {
    if constexpr (J == 0) set_m0(d.kdst);
    if constexpr (J == 4) set_m0(d.vdst);
    if constexpr (J < 4) dma16m_dev<(J & 3) * 1024>(d.voff, d.srdK, d.ksoff); else dma16m_dev<(J & 3) * 1024>(d.voff, d.srdV, d.vsoff);
  }
}
#define XBAR() do { SBAR(); asm volatile("s_waitcnt vmcnt(0) lgkmcnt(0)\n\ts_barrier" ::: "memory"); SBAR(); } while (0)

struct Ctx {
  const u16 *Qf, *Kf, *Vf; u16* Cf;
  const u16* PhiQ; const float* Part; u16* Of;
  const LAS char *lds, *lb;
  unsigned lds0;
  int lane, wid, grp, rg, head, pp, TL, L;
};
template <int MODE> DI void dma_group(Dma& d, const Ctx& c, int g) {
  const int xk = g + 2, xv = g + 1;
  if constexpr (MODE == 1) { d.kdst ^= 32768u; d.vdst ^= 32768u; d.vsoff = d.ksoff; d.ksoff += 16384u; }
  else if constexpr (MODE == 2) { d.kdst ^= 32768u; d.vdst ^= 32768u; d.vsoff = d.ksoff; d.ksoff += 32768u; }
  else {
    const bool fk = xk >= c.TL, fv = xv >= c.TL;
    const unsigned wk = (unsigned)c.wid * (fk ? 4096u : 2048u), wv = (unsigned)c.wid * (fv ? 4096u : 2048u);
    d.kdst = c.lds0 + LDS_K + (unsigned)(g & 1) * 32768u + wk; d.ksoff = (unsigned)(fk ? 2 * xk - c.TL : xk) * 16384u + wk;
    d.vdst = c.lds0 + LDS_V + (unsigned)(xv & 1) * 32768u + wv; d.vsoff = (unsigned)(fv ? 2 * xv - c.TL : xv) * 16384u + wv;
    d.k01 = xk <= c.L; d.k23 = fk && xk <= c.L; d.v01 = xv <= c.L; d.v23 = fv && xv <= c.L;
  }
}
DI const LAS char* ktile_at(const Ctx& c, int x) { const LAS char* p = c.lb + LDS_K + (x & 1) * 32768 + (x >= c.TL ? c.grp * 16384 : 0); asm volatile("" : "+v"(p)); return p; }
DI const LAS char* vtile_at(const Ctx& c, int x) { const LAS char* p = c.lb + LDS_V + (x & 1) * 32768 + (x >= c.TL ? c.grp * 16384 : 0); asm volatile("" : "+v"(p)); return p; }

template <bool FIRST, int MODE>
DI void phaseA(St& st, Til& C, const Til& P, const LAS char* kp, const LAS char* vp, const Dma& d) {
#define GA(i) do { \
    if (((i) & 3) == 0) { if (FIRST) mfma_zero(C.s[2 * ((i) >> 2)], st.fr[(i) & 7], st.q[0]); else mfma_init(C.s[2 * ((i) >> 2)], st.fr[(i) & 7], st.q[0], st.negm[0]); } \
    else mfma_acc(C.s[2 * ((i) >> 2)], st.fr[(i) & 7], st.q[(i) & 3]); \
    SBAR(); \
    if (!FIRST) {   \
      const float a_ = P.s[2 * (2 * ((i) >> 3) + (((i) & 3) >> 1)) + (((i) >> 2) & 1)][2 * ((i) & 1)], b_ = P.s[2 * (2 * ((i) >> 3) + (((i) & 3) >> 1)) + (((i) >> 2) & 1)][2 * ((i) & 1) + 1]; \
      { unsigned w_ = cvtpk(a_, b_); PIN(w_); st.pw[(i) >> 2][(i) & 3] = w_; } SBAR(); } \
    if (((i) & 3) == 0) { if (FIRST) mfma_zero(C.s[2 * ((i) >> 2) + 1], st.fr[(i) & 7], st.q[4]); else mfma_init(C.s[2 * ((i) >> 2) + 1], st.fr[(i) & 7], st.q[4], st.negm[1]); } \
    else mfma_acc(C.s[2 * ((i) >> 2) + 1], st.fr[(i) & 7], st.q[4 + ((i) & 3)]); \
    SBAR(); \
    if (((i) & 1) == 1) { if ((i) < 8) { RDK2(((i) + 8) & 15, ((i) + 7) & 15); } else if (!FIRST) { RDV2(((i) + 8) & 15, ((i) + 7) & 15); } } \
    if (FIRST) { st.o[i] = (f32x4){}; PIN(st.o[i]); } \
    if (!FIRST) { if ((i) == 0) dma_piece<MODE, 3>(d); if ((i) == 2) dma_piece<MODE, 4>(d); if ((i) == 4) dma_piece<MODE, 5>(d); if ((i) == 8) dma_piece<MODE, 6>(d); if ((i) == 10) dma_piece<MODE, 7>(d); } \
    SBAR(); } while (0)
  GA(0); GA(1); GA(2); GA(3); GA(4); GA(5); GA(6); GA(7); GA(8); GA(9); GA(10); GA(11); GA(12); GA(13); GA(14); GA(15);
  if (!FIRST) asm volatile("" ::"v"(st.negm[0]), "v"(st.negm[1]));
#undef GA
}

#define MSUM(i) do { if (((i) & 7) == 2) mfma_acc(st.ls[0], st.ones, __builtin_bit_cast(bf16x8, st.pw[2 * ((i) >> 3)])); if (((i) & 7) == 5) mfma_acc(st.ls[1], st.ones, __builtin_bit_cast(bf16x8, st.pw[2 * ((i) >> 3) + 1])); } while (0)
template <int MODE>
DI void phaseB(St& st, Til& C, const LAS char* vp, const LAS char* kn, Dma& d, const Ctx& c, int g) {
#define GB(i) do { \
    if ((i) == 11) { XBAR(); dma_group<MODE>(d, c, g); } \
    mfma_acc(st.o[2 * ((i) & 7)], st.fr[(i) & 7], __builtin_bit_cast(bf16x8, st.pw[2 * ((i) >> 3)])); \
    SBAR(); \
    { float e_ = EXP2(C.s[(i) >> 1][2 * ((i) & 1)]); PIN(e_); C.s[(i) >> 1][2 * ((i) & 1)] = e_; } \
    SBAR(); \
    mfma_acc(st.o[2 * ((i) & 7) + 1], st.fr[(i) & 7], __builtin_bit_cast(bf16x8, st.pw[2 * ((i) >> 3) + 1])); \
    SBAR(); \
    { float e_ = EXP2(C.s[(i) >> 1][2 * ((i) & 1) + 1]); PIN(e_); C.s[(i) >> 1][2 * ((i) & 1) + 1] = e_; } \
    SBAR(); \
    MSUM(i); \
    if (((i) & 1) == 1 && (i) < 8) { RDV2(((i) + 8) & 15, ((i) + 7) & 15); } \
    if ((i) == 11) { { const LAS char* kp = kn; RDK2(1, 0); SBAR(); RDK2(3, 2); } } \
    if ((i) == 12) { dma_piece<MODE, 0>(d); } \
    if ((i) == 13) { { const LAS char* kp = kn; RDK2(5, 4); } } \
    if ((i) == 14) { dma_piece<MODE, 1>(d); dma_piece<MODE, 2>(d); } \
    if ((i) == 15) { { const LAS char* kp = kn; RDK2(7, 6); } } \
    SBAR(); } while (0)
  GB(0); GB(1); GB(2); GB(3); GB(4); GB(5); GB(6); GB(7); GB(8); GB(9); GB(10); GB(11); GB(12); GB(13); GB(14); GB(15);
#undef GB
}

struct BIn { bf16x8 x[2], w0, w1; };
struct BPart { f32x4 part[8]; };
DI void b2_load_part(BPart& b, const float* __restrict__ Part, int head, int wid, int lane) {
  if (wid < 6) {
#pragma unroll
    for (int pm = 0; pm < 8; ++pm) b.part[pm] = __builtin_bit_cast(f32x4, ld128_dev(mk_rsrc(Part, 8u * 32u * 6144u), (unsigned)(((pm * 32 + head) * 6 + wid) * 64 + lane) * 16u));
  }
}
DI void b2_load(BIn& b, const u16* __restrict__ Qr, int head, int pp, int wid, int lane) {
#pragma unroll
  for (int i = 0; i < 2; ++i) b.x[i] = ld128_dev(mk_rsrc(Qr, 32u * 128u * 1024u + 32u * 2048u), (unsigned)((head * 128 + pp * 16 + wid * 2 + i) * 64 + lane) * 16u);
  b.w0 = ld128_dev(mk_rsrc(Qr, 32u * 128u * 1024u + 32u * 2048u), (unsigned)((32 * 128 + head * 2) * 64 + lane) * 16u); b.w1 = ld128_dev(mk_rsrc(Qr, 32u * 128u * 1024u + 32u * 2048u), (unsigned)((32 * 128 + head * 2 + 1) * 64 + lane) * 16u);
}
DI void b2_publish(const BPart& b, LAS char* xr, int wid, int lane) {
  if (wid < 6) { const f32x4 s = ((b.part[0] + b.part[1]) + (b.part[2] + b.part[3])) + ((b.part[4] + b.part[5]) + (b.part[6] + b.part[7]));
    *(LAS u32x2*)(xr + wid * 512 + lane * 8) = (u32x2){cvtpk(s[0], s[1]), cvtpk(s[2], s[3])}; }
}
DI void b2_finish(const BIn& b, const LAS char* xr, u16* __restrict__ Of, int head, int pp, int wid, int lane) {
  const int c = lane & 15, g = lane >> 4;
  bf16x8 q0[2], q1[2];
  { f32x4 fa[2], fb[2];
#pragma unroll
    for (int i = 0; i < 2; ++i) { fa[i] = MFMA16(b.w0, b.x[i], ((f32x4){})); fb[i] = MFMA16(b.w1, b.x[i], ((f32x4){})); }
    asm volatile("s_nop 7\n\ts_nop 1" : "+v"(fa[0]), "+v"(fa[1]), "+v"(fb[0]), "+v"(fb[1]));
    u32x4 f0[2]; u32x2 f1[2];
    hedgehog2(fa, fb, g, f0, f1);
#pragma unroll
    for (int i = 0; i < 2; ++i) { q0[i] = __builtin_bit_cast(bf16x8, f0[i]);
      auto s0 = __builtin_amdgcn_permlane16_swap(f1[i][0], f1[i][0], false, false); auto s1 = __builtin_amdgcn_permlane16_swap(f1[i][1], f1[i][1], false, false);
      const u32x4 w = g == 0 ? (u32x4){s0[0], s1[0], s0[1], s1[1]} : (u32x4){0u, 0u, 0u, 0u};
      q1[i] = __builtin_bit_cast(bf16x8, w); } }
  bf16x8 a0[2], a1[2];
#pragma unroll
  for (int db = 0; db < 2; ++db) {
    a0[db] = __builtin_shufflevector(*(const LAS s16x4*)(xr + db * 512 + lane * 8), *(const LAS s16x4*)(xr + (2 + db) * 512 + lane * 8), 0, 1, 2, 3, 4, 5, 6, 7);
    a1[db] = __builtin_shufflevector(*(const LAS s16x4*)(xr + (4 + db) * 512 + c * 8), *(const LAS s16x4*)(xr + (4 + db) * 512 + (c + 16) * 8), 0, 1, 2, 3, 4, 5, 6, 7);
    if (g != 0) a1[db] = (bf16x8){0, 0, 0, 0, 0, 0, 0, 0};
  }
#pragma unroll
  for (int i = 0; i < 2; ++i) {
    f32x4 n0 = MFMA16(a0[0], q0[i], ((f32x4){})); n0 = MFMA16(a1[0], q1[i], n0);
    f32x4 n1 = MFMA16(a0[1], q0[i], ((f32x4){})); n1 = MFMA16(a1[1], q1[i], n1);
    const float inv = __builtin_amdgcn_rcpf(__shfl(n1[0], c + 16) + 1e-6f);
    const rsrc_t ro = mk_rsrc(Of, 2048u * 640u * 2u); const unsigned base = (unsigned)(pp * 16 + wid * 2 + i) * 1024u;
    { const int k = head * 20 + 4 * g; st64<CF_AUX>((u32x2){cvtpk(n0[0] * inv, n0[1] * inv), cvtpk(n0[2] * inv, n0[3] * inv)}, ro, base + (unsigned)((k >> 5) * 131072 + (c + 16 * ((k >> 3) & 3)) * 16 + 8 * ((k >> 2) & 1))); }
    { const int k = head * 20 + 16; st64<CF_AUX>((u32x2){cvtpk(n1[0] * inv, n1[1] * inv), cvtpk(n1[2] * inv, n1[3] * inv)}, ro, g == 0 ? base + (unsigned)((k >> 5) * 131072 + (c + 16 * ((k >> 3) & 3)) * 16 + 8 * ((k >> 2) & 1)) : 0x80000000u); }
  }
}

#define PIN_Q() asm volatile("" : "+v"(st.q[0]), "+v"(st.q[1]), "+v"(st.q[2]), "+v"(st.q[3]), "+v"(st.q[4]), "+v"(st.q[5]), "+v"(st.q[6]), "+v"(st.q[7]))
#define PIN_O() do { asm volatile("" : "+v"(st.o[0]), "+v"(st.o[1]), "+v"(st.o[2]), "+v"(st.o[3]), "+v"(st.o[4]), "+v"(st.o[5]), "+v"(st.o[6]), "+v"(st.o[7])); \
    asm volatile("s_nop 1" : "+v"(st.o[8]), "+v"(st.o[9]), "+v"(st.o[10]), "+v"(st.o[11]), "+v"(st.o[12]), "+v"(st.o[13]), "+v"(st.o[14]), "+v"(st.o[15]), "+v"(st.ls[0]), "+v"(st.ls[1])); } while (0)
constexpr int LDS_MX = LDS_V + 32768 + 16384;

DI bool light_epilogue(St& st, const Ctx& c) {
  MFMA_SETTLE();
  const float l0 = st.ls[0][0], l1 = st.ls[1][0];
  const float f[2] = {__builtin_amdgcn_rcpf(l0), __builtin_amdgcn_rcpf(l1)};
  f32x2 chk = {0.f, 0.f};
  const rsrc_t r = mk_rsrc(c.Cf, 2048u * 4096u * 2u);
  const int lane = c.lane;
  const unsigned vo = (unsigned)((c.head * 4 * 128 + c.pp * 8 + c.rg * 2) * 64 + ((lane & 15) + 16 * ((((lane >> 4) & 1) << 1) | (lane >> 5)))) * 16u;
#pragma unroll
  for (int t = 0; t < 4; ++t)
#pragma unroll
    for (int qb = 0; qb < 2; ++qb) { const f32x4 x = st.o[4 * t + qb] * f[qb], y = st.o[4 * t + 2 + qb] * f[qb];
      chk = __builtin_elementwise_fma((f32x2){x[0], x[1]}, (f32x2){0.f, 0.f}, chk); chk = __builtin_elementwise_fma((f32x2){x[2], x[3]}, (f32x2){0.f, 0.f}, chk);
      chk = __builtin_elementwise_fma((f32x2){y[0], y[1]}, (f32x2){0.f, 0.f}, chk); chk = __builtin_elementwise_fma((f32x2){y[2], y[3]}, (f32x2){0.f, 0.f}, chk);
      st128<CF_AUX>(pair_swap(cvtpk(x[0], x[1]), cvtpk(x[2], x[3]), cvtpk(y[0], y[1]), cvtpk(y[2], y[3])), r, vo + (unsigned)(t * 128 + qb) * 1024u); SBAR(); }
#pragma unroll
  for (int i = 0; i < 16; ++i) st.o[i] = (f32x4){};
  st.ls[0] = (f32x4){}; st.ls[1] = (f32x4){};
  PIN_O();
  return __any(!(l0 < L_LIMIT) || !(l1 < L_LIMIT) || !(chk[0] == 0.f) || !(chk[1] == 0.f));
}

DI void phase2(const u16* __restrict__ Qf, const u16* __restrict__ Kf, const u16* __restrict__ Vf, u16* __restrict__ Cf,
               const u16* __restrict__ PhiQ, const float* __restrict__ Part, u16* __restrict__ Of, tag_t* __restrict__ flags, tag_t tag, unsigned long long* dbg) {
  extern __shared__ __attribute__((aligned(16))) char smem[];
  Ctx c; c.Qf = Qf; c.Kf = Kf; c.Vf = Vf; c.Cf = Cf; c.PhiQ = PhiQ; c.Part = Part; c.Of = Of;
  c.lds = (const LAS char*)smem; c.lds0 = (unsigned)(uintptr_t)c.lds;
  const int tid = threadIdx.x; c.lane = tid & 63;
  c.wid = (int)rfl(tid >> 6); c.grp = c.wid >> 2; c.rg = c.wid & 3;
  const int xcd = blockIdx.x & 7; int hl, item; item_of((int)(blockIdx.x >> 3), hl, item); c.head = xcd * 4 + hl;
  const int pp = (int)((ITEM_LIGHT >> (4 * item)) & 15u), blkH = (int)((ITEM_HEAVY >> (4 * item)) & 15u);
  c.pp = pp; c.TL = 2 * pp + 2; c.L = pp + blkH + 1; c.lb = c.lds + c.lane * 16;
  const int lane = c.lane, wid = c.wid, grp = c.grp, rg = c.rg, TL = c.TL, L = c.L;
#define AST(i)
  Dma d; d.voff = (unsigned)lane * 16u;
  d.srdK = mk_rsrc(Kf + (size_t)c.head * 262144, 524288u); d.srdV = mk_rsrc(Vf + (size_t)c.head * 262144, 524288u);
  St st;
  st.ones = (bf16x8){0x3F80, 0x3F80, 0x3F80, 0x3F80, 0x3F80, 0x3F80, 0x3F80, 0x3F80}; PIN(st.ones);
  const int pn1 = (c.head >> 1) & 1;
  { int line = -1;
    if (lane < 2) line = FL_U0 + xcd + 8 * ((((lane == 0 ? blkH : pp) >> 1) << 1) | pn1);
    else if (lane < 10) { const int k = ((lane - 2) >> 1) * 4 + ((lane & 1) << 1) + pn1; line = FL_P1 + xcd + 8 * (16 + (k & 7)); }
    flag_wait(flags, line, tag); }
  const int late_line = lane < 8 ? FL_P1 + xcd + 8 * ((lane >> 1) * 4 + ((lane & 1) << 1) + pn1) : -1;
  const int t_late = 6 + TL / 2;
  { const unsigned w2 = (unsigned)wid * 2048u;
    set_m0(c.lds0 + LDS_K + w2); dma16m_dev<0>(d.voff, d.srdK, w2); dma16m_dev<1024>(d.voff, d.srdK, w2);
    { const rsrc_t rq0 = mk_rsrc(Qf + (size_t)c.head * 262144, 524288u); const unsigned qo = (unsigned)((grp == 0 ? blkH : pp) * 4 + rg) * 8192u + (unsigned)lane * 16u;
#pragma unroll
      for (int i = 0; i < 8; ++i) st.q[i] = ld128_dev(rq0, qo + i * 1024u); }
    PIN_Q();
    XBAR();
    set_m0(c.lds0 + LDS_V + w2); dma16m_dev<0>(d.voff, d.srdV, w2); dma16m_dev<1024>(d.voff, d.srdV, w2);
    set_m0(c.lds0 + LDS_K + 32768 + w2); dma16m_dev<0>(d.voff, d.srdK, 16384u + w2); dma16m_dev<1024>(d.voff, d.srdK, 16384u + w2);
    if (grp == 1) { const rsrc_t rq = mk_rsrc(Qf + (size_t)c.head * 262144, 524288u); const unsigned so = (unsigned)(blkH * 4 + rg) * 8192u;
      set_m0(c.lds0 + LDS_X + rg * 8192); dma16m_dev<0>(d.voff, rq, so); dma16m_dev<1024>(d.voff, rq, so); dma16m_dev<2048>(d.voff, rq, so); dma16m_dev<3072>(d.voff, rq, so);
      set_m0(c.lds0 + LDS_X + rg * 8192 + 4096); dma16m_dev<0>(d.voff, rq, so + 4096u); dma16m_dev<1024>(d.voff, rq, so + 4096u); dma16m_dev<2048>(d.voff, rq, so + 4096u); dma16m_dev<3072>(d.voff, rq, so + 4096u); } }
  AST(0);
  const int thr = rg * 32 + (lane & 15) - 8 * (lane >> 4);
  Til a, b;
  { const LAS char* kp = ktile_at(c, 0);
    A_RDK(1); A_RDK(0); A_RDK(3); A_RDK(2); A_RDK(5); A_RDK(4); A_RDK(7); A_RDK(6);
    phaseA<true, 0>(st, a, a, kp, kp, d);
    MFMA_SETTLE();
    if (grp == 1 && TL == 2) cmask(a, thr);
#pragma unroll
    for (int qb = 0; qb < 2; ++qb) {
      float mx = max3f(a.s[qb][0], a.s[qb][1], a.s[qb][2]), my = max3f(a.s[qb][3], a.s[2 + qb][0], a.s[2 + qb][1]);
      mx = max3f(mx, a.s[2 + qb][2], a.s[2 + qb][3]); my = max3f(my, a.s[4 + qb][0], a.s[4 + qb][1]);
      mx = max3f(mx, a.s[4 + qb][2], a.s[4 + qb][3]); my = max3f(my, a.s[6 + qb][0], a.s[6 + qb][1]);
      mx = max3f(mx, a.s[6 + qb][2], a.s[6 + qb][3]); mx = max3f(mx, my, -1e30f);
      asm volatile("s_nop 1" : "+v"(mx));
      st.m[qb] = xrow_max(mx); st.ls[qb] = (f32x4){};
    }
    if (grp == 0) *(LAS f32x2*)((LAS char*)c.lds + LDS_MX + rg * 512 + lane * 8) = (f32x2){st.m[0], st.m[1]};
    XBAR();
    dma_group<0>(d, c, 0);
    dma_piece<0, 0>(d); dma_piece<0, 1>(d); dma_piece<0, 2>(d);
    { const LAS char* kp = ktile_at(c, 1); A_RDK(1); A_RDK(0); A_RDK(3); A_RDK(2); A_RDK(5); A_RDK(4); A_RDK(7); A_RDK(6); }
#pragma unroll
    for (int qb = 0; qb < 2; ++qb) { st.negm[qb] = (f32x4){-st.m[qb], -st.m[qb], -st.m[qb], -st.m[qb]};
#pragma unroll
      for (int kb = 0; kb < 4; ++kb)
#pragma unroll
        for (int r = 0; r < 4; ++r) a.s[2 * kb + qb][r] = EXP2(a.s[2 * kb + qb][r] - st.m[qb]); }
    asm volatile("s_nop 1" : "+v"(st.negm[0]), "+v"(st.negm[1]), "+v"(st.ls[0]), "+v"(st.ls[1]));
  }
  AST(1);
#define HOT(MODE, C, P, t, kp_, vp_, kn_) do { phaseA<false, MODE>(st, C, P, kp_, vp_, d); SBAR(); phaseB<MODE>(st, C, vp_, kn_, d, c, t); } while (0)
#define GEN(C, P, t) do { \
    phaseA<false, 0>(st, C, P, ktile_at(c, t), vtile_at(c, (t) - 1), d); \
    if (__builtin_expect((grp == 1 && (t) >= TL - 2 && (t) < TL) || (t) == L, 0)) { MFMA_SETTLE(); cmask(C, (t) == L ? thr - grp * 64 : ((t) == TL - 1 ? thr - 64 : thr)); } \
    SBAR(); \
    phaseB<0>(st, C, vtile_at(c, (t) - 1), ktile_at(c, (t) + 1), d, c, t); } while (0)
  {
    const LAS char *kO = c.lb + LDS_K + 32768, *kE = c.lb + LDS_K, *vO = c.lb + LDS_V + 32768, *vE = c.lb + LDS_V;
    asm volatile("" : "+v"(kO), "+v"(kE), "+v"(vO), "+v"(vE));
    for (int t = 1; t + 3 < TL; t += 2) { HOT(1, b, a, t, kO, vE, kE); HOT(1, a, b, t + 1, kE, vO, kO); }
  }
  bool waited = false;
#define LATE(tmax) do { if (!waited && (tmax) >= t_late) { flag_wait(flags, late_line, tag); waited = true; } } while (0)
  LATE(TL);
  if (TL >= 4) { GEN(b, a, TL - 3); GEN(a, b, TL - 2); MFMA_SETTLE(); }
  GEN(b, a, TL - 1);
  if (grp == 1) {
    const LAS char* qs = c.lb + LDS_X + rg * 8192;
#pragma unroll
    for (int i = 0; i < 8; ++i) st.q[i] = lds_rd128(qs + i * 1024);
    const f32x2 mh = *(const LAS f32x2*)(c.lds + LDS_MX + rg * 512 + lane * 8);
    st.negm[0] = (f32x4){-mh[0], -mh[0], -mh[0], -mh[0]}; st.negm[1] = (f32x4){-mh[1], -mh[1], -mh[1], -mh[1]};
    PIN_Q(); asm volatile("s_nop 1" : "+v"(st.negm[0]), "+v"(st.negm[1]));
  }
  SBAR();
  GEN(a, b, TL);
  bool badL = false;
  if (grp == 1) badL = light_epilogue(st, c);
  SBAR();
  {
    const unsigned go = (unsigned)grp * 16384u;
    const LAS char *kO = c.lb + LDS_K + 32768 + go, *kE = c.lb + LDS_K + go, *vO = c.lb + LDS_V + 32768 + go, *vE = c.lb + LDS_V + go;
    asm volatile("" : "+v"(kO), "+v"(kE), "+v"(vO), "+v"(vE));
    int t = TL + 1;
    for (; t + 3 <= L; t += 2) { LATE(t + 1); HOT(2, b, a, t, kO, vE, kE); HOT(2, a, b, t + 1, kE, vO, kO); }
    LATE(L);
    if (t + 1 <= L) { GEN(b, a, t); GEN(a, b, t + 1); MFMA_SETTLE(); t += 2; }
    if (t <= L) { GEN(b, a, t); MFMA_SETTLE(); a = b; }
  }
#undef LATE
#undef GEN
#undef HOT
  AST(2);
  BIn bi; BPart bp;
  flag_wait(flags, lane < 8 ? FL_P1 + xcd + 8 * (24 + lane) : -1, tag);
  b2_load_part(bp, c.Part, c.head, wid, lane); b2_load(bi, c.PhiQ, c.head, item, wid, lane);
  { const LAS char* vp = vtile_at(c, L);
    A_RDV(1); A_RDV(0); A_RDV(3); A_RDV(2); A_RDV(5); A_RDV(4); A_RDV(7); A_RDV(6);
#pragma unroll
    for (int i = 0; i < 16; ++i) { const float x = a.s[2 * (2 * (i >> 3) + ((i & 3) >> 1)) + ((i >> 2) & 1)][2 * (i & 1)], y = a.s[2 * (2 * (i >> 3) + ((i & 3) >> 1)) + ((i >> 2) & 1)][2 * (i & 1) + 1];
      st.pw[i >> 2][i & 3] = cvtpk(x, y); }
    asm volatile("s_nop 1" : "+v"(st.pw[0]), "+v"(st.pw[1]), "+v"(st.pw[2]), "+v"(st.pw[3]));
#define GD(i) do { mfma_acc(st.o[2 * ((i) & 7)], st.fr[(i) & 7], __builtin_bit_cast(bf16x8, st.pw[2 * ((i) >> 3)])); \
      mfma_acc(st.o[2 * ((i) & 7) + 1], st.fr[(i) & 7], __builtin_bit_cast(bf16x8, st.pw[2 * ((i) >> 3) + 1])); MSUM(i); \
      if (((i) & 1) == 1 && (i) < 8) { RDV2(((i) + 8) & 15, ((i) + 7) & 15); } SBAR(); } while (0)
    GD(0); GD(1); GD(2); GD(3); GD(4); GD(5); GD(6); GD(7); GD(8); GD(9); GD(10); GD(11); GD(12); GD(13); GD(14); GD(15);
    MFMA_SETTLE();
#undef GD
  }
  const float l[2] = {st.ls[0][0], st.ls[1][0]};
  LAS char* xr = (LAS char*)c.lds + LDS_X;
  LAS char* sendp = xr + wid * 4096 + lane * 16;
  const LAS char* recvp = xr + (wid ^ 4) * 4096 + lane * 16;
  LAS char* aux = (LAS char*)c.lds + LDS_K + ((L + 1) & 1) * 32768;
  f32x4 k[8];
#define PACK8(x, y) (u32x4){cvtpk((x)[0], (x)[1]), cvtpk((x)[2], (x)[3]), cvtpk((y)[0], (y)[1]), cvtpk((y)[2], (y)[3])}
  if (grp == 0) {
#pragma unroll
    for (int i = 0; i < 4; ++i) *(LAS u32x4*)(sendp + i * 1024) = PACK8(st.o[8 + 4 * (i >> 1) + (i & 1)], st.o[8 + 4 * (i >> 1) + 2 + (i & 1)]);
#pragma unroll
    for (int i = 0; i < 8; ++i) k[i] = st.o[i];
  } else {
#pragma unroll
    for (int i = 0; i < 4; ++i) *(LAS u32x4*)(sendp + i * 1024) = PACK8(st.o[4 * (i >> 1) + (i & 1)], st.o[4 * (i >> 1) + 2 + (i & 1)]);
#pragma unroll
    for (int i = 0; i < 8; ++i) k[i] = st.o[8 + i];
  }
  *(LAS f32x2*)(aux + wid * 512 + lane * 8) = (f32x2){l[0], l[1]};
  b2_publish(bp, aux + 8192, wid, lane);
  asm volatile("s_waitcnt lgkmcnt(0)\n\ts_barrier" ::: "memory");
  const f32x2 ol = *(const LAS f32x2*)(aux + (wid ^ 4) * 512 + lane * 8);
  bool bad = !(l[0] < L_LIMIT) || !(l[1] < L_LIMIT) || !(ol[0] < L_LIMIT) || !(ol[1] < L_LIMIT);
  const float fs[2] = {__builtin_amdgcn_rcpf(l[0] + ol[0]), __builtin_amdgcn_rcpf(l[1] + ol[1])};
  f32x2 chk = {0.f, 0.f};
  u32x4 res[4];
#pragma unroll
  for (int i = 0; i < 4; ++i) {
    const u32x4 x = *(const LAS u32x4*)(recvp + i * 1024);
    const f32x4 p = k[4 * (i >> 1) + (i & 1)], q = k[4 * (i >> 1) + 2 + (i & 1)]; const float f = fs[i & 1];
    const float v0 = (p[0] + bf_lo(x[0])) * f, v1 = (p[1] + bf_hi(x[0])) * f, v2 = (p[2] + bf_lo(x[1])) * f, v3 = (p[3] + bf_hi(x[1])) * f;
    const float v4 = (q[0] + bf_lo(x[2])) * f, v5 = (q[1] + bf_hi(x[2])) * f, v6 = (q[2] + bf_lo(x[3])) * f, v7 = (q[3] + bf_hi(x[3])) * f;
    chk = __builtin_elementwise_fma((f32x2){v0, v1}, (f32x2){0.f, 0.f}, chk); chk = __builtin_elementwise_fma((f32x2){v2, v3}, (f32x2){0.f, 0.f}, chk);
    chk = __builtin_elementwise_fma((f32x2){v4, v5}, (f32x2){0.f, 0.f}, chk); chk = __builtin_elementwise_fma((f32x2){v6, v7}, (f32x2){0.f, 0.f}, chk);
    res[i] = pair_swap(cvtpk(v0, v1), cvtpk(v2, v3), cvtpk(v4, v5), cvtpk(v6, v7));
  }
  bad = bad || !(chk[0] == 0.f) || !(chk[1] == 0.f);
  if (__builtin_expect(__any(bad), 0)) {
    safe_path(Qf, Kf, Vf, Cf, c.head, blkH, rg, grp, lane);
  } else {
#pragma unroll
    for (int i = 0; i < 4; ++i) st128<CF_AUX>(res[i], mk_rsrc(Cf, 2048u * 4096u * 2u), (unsigned)(((c.head * 4 + grp * 2 + (i >> 1)) * 128 + blkH * 8 + rg * 2 + (i & 1)) * 64 + ((lane & 15) + 16 * ((((lane >> 4) & 1) << 1) | (lane >> 5)))) * 16u);
  }
  if (__builtin_expect(badL, 0)) {
    asm volatile("s_waitcnt vmcnt(0)" ::: "memory");
    safe_path(Qf, Kf, Vf, Cf, c.head, pp, rg, 0, lane); safe_path(Qf, Kf, Vf, Cf, c.head, pp, rg, 1, lane);
  }
  b2_finish(bi, aux + 8192, Of, c.head, item, wid, lane);
  asm volatile("s_waitcnt vmcnt(0) lgkmcnt(0)\n\ts_barrier" ::: "memory");
  if (tid == 0) flag_set(flags, FL_P2 + (int)blockIdx.x, tag);
}
}

namespace outp {
constexpr int SLOTB = 40960, LDS_BYTES = 4 * SLOTB;
struct Src { rsrc_t X, W; unsigned sX, sW; };
struct XSet { bf16x8 f[2][2]; };
struct WSet { bf16x8 f[10]; };
DI void ldx(bf16x8& d, unsigned voff, rsrc_t r, unsigned soff) { asm volatile(VM_PAD "buffer_load_dwordx4 %0, %1, %2, %3 offen sc1" : "=v"(d) : "v"(voff), "s"(r), "s"(soff) : "memory"); }
DI void ldx1k(bf16x8& d, unsigned voff, rsrc_t r, unsigned soff) { asm volatile(VM_PAD "buffer_load_dwordx4 %0, %1, %2, %3 offen offset:1024 sc1" : "=v"(d) : "v"(voff), "s"(r), "s"(soff) : "memory"); }
#define RW(set, p, ks, n) (set.f[n] = lds_rd128((p) + (ks) * 10240 + (n) * 1024))
template <int I> DI void wp(const Src& s, unsigned ldsw, unsigned voff) {
  if constexpr (I == 0 || I == 2) set_m0(ldsw);
  if constexpr (I == 4) set_m0(ldsw + 4096);
  dma16m_dev<(I & 3) * 1024>(voff, s.W, s.sW + (I >> 2) * 4096u);
}
DI void round(f32x4 (&acc)[2][10], XSet& X, WSet& wA, WSet& wB, const LAS char* wc, const LAS char* wn_, Src& src, const Src& srcA, unsigned lw2, unsigned lw3, unsigned voff,
              bool h2, bool h3, int wait, bool swx, bool sww) {
#define MM(k, W_, m, n) do { mfma_acc(acc[m][n], W_.f[n], X.f[k][m]); SBAR(); } while (0)
#define HX(k, m) do { if (h2) { if (m) ldx1k(X.f[k][1], voff, src.X, src.sX + (k) * 262144u); else ldx(X.f[k][0], voff, src.X, src.sX + (k) * 262144u); } SBAR(); } while (0)
#define HW2(i) do { if (h2) wp<i>(src, lw2, voff); SBAR(); } while (0)
#define HW3(i) do { if (h3) wp<i>(src, lw3, voff); SBAR(); } while (0)
#define NOP_() do { } while (0)
#define KSTEP(k, CUR, NXT, p, A, B, C, D) do { \
    MM(k, CUR, 0, 0); RW(NXT, p, 0, 0); SBAR(); MM(k, CUR, 1, 0); MM(k, CUR, 0, 1); RW(NXT, p, 0, 1); SBAR(); MM(k, CUR, 1, 1); A; MM(k, CUR, 0, 2); RW(NXT, p, 0, 2); SBAR(); MM(k, CUR, 1, 2); \
    MM(k, CUR, 0, 3); RW(NXT, p, 0, 3); SBAR(); MM(k, CUR, 1, 3); B; MM(k, CUR, 0, 4); RW(NXT, p, 0, 4); SBAR(); MM(k, CUR, 1, 4); MM(k, CUR, 0, 5); RW(NXT, p, 0, 5); SBAR(); MM(k, CUR, 1, 5); C; \
    MM(k, CUR, 0, 6); RW(NXT, p, 0, 6); SBAR(); MM(k, CUR, 1, 6); MM(k, CUR, 0, 7); RW(NXT, p, 0, 7); SBAR(); MM(k, CUR, 1, 7); D; MM(k, CUR, 0, 8); RW(NXT, p, 0, 8); SBAR(); MM(k, CUR, 1, 8); \
    MM(k, CUR, 0, 9); RW(NXT, p, 0, 9); SBAR(); MM(k, CUR, 1, 9); } while (0)
  if (swx) src.sX = srcA.sX, src.X = srcA.X;
  KSTEP(0, wA, wB, wc + 20480, HW2(2), HW2(3), HW2(4), NOP_());
  HX(0, 0);
  if (h2) src.sW += 4u * 40960u;
  if (wait == 8) asm volatile("s_waitcnt vmcnt(8) lgkmcnt(0)\n\ts_barrier" ::: "memory");
  else if (wait == 4) asm volatile("s_waitcnt vmcnt(4) lgkmcnt(0)\n\ts_barrier" ::: "memory");
  else asm volatile("s_waitcnt vmcnt(0) lgkmcnt(0)\n\ts_barrier" ::: "memory");
  SBAR();
  if (sww) src.sW = srcA.sW, src.W = srcA.W;
  KSTEP(1, wB, wA, wn_, HX(0, 1), HW3(0), HW3(1), NOP_());
  HX(1, 0); HX(1, 1);
  if (h2) src.sX += 4u * 131072u;
#undef NOP_
#undef HW3
#undef HW2
#undef HX
#undef KSTEP
#undef MM
}
#define PIN_X(XS) asm volatile("" : "+v"(XS.f[0][0]), "+v"(XS.f[0][1]), "+v"(XS.f[1][0]), "+v"(XS.f[1][1]))
#define PIN_ACC() do { asm volatile("" : "+v"(acc[0][0]), "+v"(acc[0][1]), "+v"(acc[0][2]), "+v"(acc[0][3]), "+v"(acc[0][4]), "+v"(acc[0][5]), "+v"(acc[0][6]), "+v"(acc[0][7]), "+v"(acc[0][8]), "+v"(acc[0][9])); \
    asm volatile("s_nop 1" : "+v"(acc[1][0]), "+v"(acc[1][1]), "+v"(acc[1][2]), "+v"(acc[1][3]), "+v"(acc[1][4]), "+v"(acc[1][5]), "+v"(acc[1][6]), "+v"(acc[1][7]), "+v"(acc[1][8]), "+v"(acc[1][9])); SBAR(); } while (0)
DI u32x2 pack4(const f32x4& a) { return (u32x2){cvtpk(a[0], a[1]), cvtpk(a[2], a[3])}; }
DI f32x4 unpack4(unsigned x, unsigned y) { return (f32x4){bf_lo(x), bf_hi(x), bf_lo(y), bf_hi(y)}; }

DI void phase3(const u16* __restrict__ Cf, const u16* __restrict__ Of, const u16* __restrict__ WfA, const u16* __restrict__ WfB,
               u16* __restrict__ out, u16* __restrict__ Ex, tag_t* __restrict__ flags, tag_t tag, unsigned long long* dbg) {
#define OST(i)
  extern __shared__ __attribute__((aligned(16))) char smem[];
  const LAS char* lds = (const LAS char*)smem;
  const int tid = threadIdx.x, lane = tid & 63, wid = (int)rfl(tid >> 6), par = wid >> 2, wm = wid & 3;
  const int x = blockIdx.x & 7, j = blockIdx.x >> 3, kq = x >> 1, pm = 8 * (x & 1) + (j >> 2), pn = j & 3, tile = pm * 4 + pn;
  const unsigned ldsw = (unsigned)(uintptr_t)lds + (unsigned)wid * 5120u, voff = (unsigned)lane * 16u;
  const int a0 = kq == 0 ? 0 : (kq == 1 ? 3 : (kq == 2 ? 13 : 23)), R = kq == 0 ? 8 : (kq == 3 ? 9 : 10);
  const unsigned ox = (unsigned)(par * 128 + pm * 8 + wm * 2) * 1024u, ow = (unsigned)((wid >> 1) * 40 + pn * 10 + (wid & 1) * 5) * 1024u;
  Src srcA, src;
  srcA.X = mk_rsrc(Cf, 2048u * 4096u * 2u); srcA.W = mk_rsrc(WfA, 4096u * 640u * 2u);
  srcA.sX = (unsigned)(a0 * 4 * 128) * 1024u + ox; srcA.sW = (unsigned)(a0 * 4 * 40) * 1024u + ow;
  src.X = mk_rsrc(kq == 0 ? Of : Cf, kq == 0 ? 2048u * 640u * 2u : 2048u * 4096u * 2u); src.W = mk_rsrc(kq == 0 ? WfB : WfA, kq == 0 ? 640u * 640u * 2u : 4096u * 640u * 2u);
  src.sX = kq == 0 ? ox : srcA.sX; src.sW = kq == 0 ? ow : srcA.sW;
  XSet X0, X1;
  flag_wait(flags, FL_P1 + (lane & 7) + 8 * (24 + (lane >> 3)), tag);
  { Src w0 = src;
    wp<0>(w0, ldsw, voff); wp<1>(w0, ldsw, voff); wp<2>(w0, ldsw, voff); wp<3>(w0, ldsw, voff); wp<4>(w0, ldsw, voff); w0.sW += 4u * 40960u;
    wp<0>(w0, ldsw + SLOTB, voff); wp<1>(w0, ldsw + SLOTB, voff); wp<2>(w0, ldsw + SLOTB, voff); wp<3>(w0, ldsw + SLOTB, voff); wp<4>(w0, ldsw + SLOTB, voff); w0.sW += 4u * 40960u;
    wp<0>(w0, ldsw + 2 * SLOTB, voff); wp<1>(w0, ldsw + 2 * SLOTB, voff); src.sW = w0.sW; }
  {
    const int RA = kq == 0 ? 3 : R, it = (int)((attn::BLOCK_ITEM >> (4 * pm)) & 15ull); int line = -1;
    if (lane < RA) line = FL_P2 + attn::wg_of(a0 + lane, it);
    else if (kq == 0 && lane >= 32) line = FL_P2 + attn::wg_of(lane - 32, pm >> 1);
    flag_wait(flags, line, tag); }
#define FETCHX(XS) do { _Pragma("unroll") for (int k = 0; k < 2; ++k) { ldx(XS.f[k][0], voff, src.X, src.sX + k * 262144u); ldx1k(XS.f[k][1], voff, src.X, src.sX + k * 262144u); } src.sX += 4u * 131072u; } while (0)
  FETCHX(X0); FETCHX(X1);
#undef FETCHX
  const LAS char* wa = lds + par * 10240 + lane * 16;
  f32x4 acc[2][10];
#pragma unroll
  for (int m = 0; m < 2; ++m)
#pragma unroll
    for (int n = 0; n < 10; ++n) acc[m][n] = (f32x4){};
  PIN_ACC();
  WSet wA, wB;
  asm volatile("s_waitcnt vmcnt(4)\n\ts_barrier" ::: "memory"); SBAR();
  OST(0);
#pragma unroll
  for (int n = 0; n < 10; ++n) RW(wA, wa, 0, n);
  const int c = lane & 15, g = lane >> 4;
  const rsrc_t rout = mk_rsrc(out, 2048u * 1280u * 2u);
  const unsigned vout = (unsigned)((pm * 128 + wm * 32 + par * 16 + c) * 2560 + (pn * 160 + 4 * g) * 2);
  u32x2 bres[10];
#pragma unroll
  for (int t = 0; t < 10; ++t) bres[t] = (u32x2){0u, 0u};
  unsigned o0 = 0, o1 = SLOTB, o2 = 2 * SLOTB;
  for (int s = 0; s < R; s += 2) {
    PIN_X(X0);
    round(acc, X0, wA, wB, wa + o0, wa + o1, src, srcA, ldsw + o2, ldsw + o0, voff, s + 2 < R, s + 3 < R, s == 0 ? 4 : (s + 2 < R ? 8 : 0), false, kq == 0 && s == 2);
    if (kq == 0 && s == 4) {
      MFMA_SETTLE();
      LAS char* mw = (LAS char*)lds + 3 * SLOTB + wid * 5120 + lane * 8; const LAS char* mr = lds + 3 * SLOTB + (wid ^ 4) * 5120 + lane * 8;
#pragma unroll
      for (int n = 0; n < 10; ++n) *(LAS u32x2*)(mw + n * 512) = pack4(par == 0 ? acc[1][n] : acc[0][n]);
      asm volatile("s_waitcnt lgkmcnt(0)\n\ts_barrier" ::: "memory");
#pragma unroll
      for (int n = 0; n < 10; ++n) { const u32x2 o = *(const LAS u32x2*)(mr + n * 512); bres[n] = pack4((par == 0 ? acc[0][n] : acc[1][n]) + unpack4(o[0], o[1])); acc[0][n] = (f32x4){}; acc[1][n] = (f32x4){}; }
      PIN_ACC();
    }
    if (s + 1 < R) {
      PIN_X(X1);
      round(acc, X1, wA, wB, wa + o1, wa + o2, src, srcA, ldsw + o0, ldsw + o1, voff, s + 3 < R, s + 4 < R, s + 3 < R ? 8 : 0, kq == 0 && s == 2, false);
    }
    { const unsigned t = o0; o0 = o2; o2 = o1; o1 = t; }
  }
  MFMA_SETTLE();
  OST(1);
  f32x4 own[10];
  { const unsigned da = o0, db = (R & 1) ? o2 : o1;
    LAS char* mw = (LAS char*)lds + (par == 0 ? da : db) + wm * 10240 + lane * 16; const LAS char* mr = lds + (par == 0 ? db : da) + wm * 10240 + lane * 16;
#pragma unroll
    for (int n = 0; n < 10; ++n) { *(LAS f32x4*)(mw + n * 1024) = par == 0 ? acc[1][n] : acc[0][n]; own[n] = par == 0 ? acc[0][n] : acc[1][n]; }
    asm volatile("s_waitcnt lgkmcnt(0)\n\ts_barrier" ::: "memory");
#pragma unroll
    for (int n = 0; n < 10; ++n) own[n] += *(const LAS f32x4*)(mr + n * 1024);
  }
  const rsrc_t rex = mk_rsrc(Ex, 64u * 4u * 8u * 4u * 2048u);
  { const unsigned sb = (unsigned)(((tile * 4 + kq) * 8 + wid) * 4) * 2048u;
#define SEND(o) do { if (kq != (o)) { const u32x2 p0 = pack4(own[o]), p1 = pack4(own[(o) + 4]); st128<16>((u32x4){p0[0], p0[1], p1[0], p1[1]}, rex, sb + (o) * 2048u + lane * 16u); \
      if ((o) < 2) st64<16>(pack4(own[((o) + 8) % 10]), rex, sb + (o) * 2048u + 1024u + lane * 8u); } } while (0)
    SEND(0); SEND(1); SEND(2); SEND(3);
#undef SEND
  }
  asm volatile("s_waitcnt vmcnt(0)" ::: "memory");
  OST(2);
  tag_t* fl = flags + (FL_X + tile * 8 + wid) * 8;
  if (lane == 0) __hip_atomic_store(fl + kq, tag, RLX_AGENT);
  if (kq == 0) {
#pragma unroll
    for (int n = 0; n < 10; ++n) st64<0>(bres[n], rout, vout + 1280u + n * 32u);
  }
  { const unsigned long long t0 = __builtin_amdgcn_s_memrealtime();
    for (;;) {
      const tag_t v = lane < 4 ? __hip_atomic_load(fl + lane, RLX_AGENT) : tag;
      if (__all(v == tag)) break;
      if (__builtin_amdgcn_s_memrealtime() - t0 > 200000ull) break;
      __builtin_amdgcn_s_sleep(1);
    }
    asm volatile("" ::: "memory");
  }
  OST(3);
  if (blockIdx.x == 0 && tid == 0) flag_set(flags, FL_EPOCH, tag);
  u32x4 rp[3]; u32x2 rs[3];
#pragma unroll
  for (int i = 0; i < 3; ++i) { const int src_ = (kq + 1 + i) & 3; const unsigned sb = (unsigned)(((tile * 4 + src_) * 8 + wid) * 4 + kq) * 2048u;
    rp[i] = __builtin_amdgcn_raw_buffer_load_b128(rex, sb + lane * 16u, 0, 16);
    rs[i] = __builtin_amdgcn_raw_buffer_load_b64(rex, kq < 2 ? sb + 1024u + lane * 8u : 0x80000000u, 0, 16); }
  f32x4 f0 = kq == 0 ? own[0] : kq == 1 ? own[1] : kq == 2 ? own[2] : own[3], f1 = kq == 0 ? own[4] : kq == 1 ? own[5] : kq == 2 ? own[6] : own[7], f2 = kq == 0 ? own[8] : own[9];
#pragma unroll
  for (int i = 0; i < 3; ++i) { f0 += unpack4(rp[i][0], rp[i][1]); f1 += unpack4(rp[i][2], rp[i][3]); f2 += unpack4(rs[i][0], rs[i][1]); }
  st64<0>(pack4(f0), rout, vout + (unsigned)kq * 32u); st64<0>(pack4(f1), rout, vout + (unsigned)(kq + 4) * 32u); st64<0>(pack4(f2), rout, kq < 2 ? vout + (unsigned)(kq + 8) * 32u : 0x80000000u);
}
}

#define DBGPARAM
#define DBGARG
#define DBG(i) nullptr
__global__ __launch_bounds__(512, 2) void fused_kernel(const u16* __restrict__ Ax, const u16* __restrict__ ckv, const u16* __restrict__ wq, const u16* __restrict__ wuk, const u16* __restrict__ wuv,
                                                       const u16* __restrict__ wo, const u16* __restrict__ Bx, const u16* __restrict__ wqkv, const u16* __restrict__ wphi, const u16* __restrict__ Bwo,
                                                       u16* __restrict__ out, char* __restrict__ ws DBGPARAM) {
  u16* Qf = (u16*)(ws); u16* Kf = (u16*)(ws + (16u << 20)); u16* Vf = (u16*)(ws + (32u << 20)); u16* Cf = (u16*)(ws + (48u << 20));
  u16* PhiQ = (u16*)(ws + (64u << 20)); u16* Of = (u16*)(ws + (72u << 20)); float* Part = (float*)(ws + (76u << 20));
  u16* WfA = (u16*)(ws + (80u << 20)); u16* WfB = (u16*)(ws + (86u << 20)); tag_t* flags = (tag_t*)(ws + (88u << 20)); u16* Ex = (u16*)(ws + (90u << 20));
  const tag_t e0 = __hip_atomic_load(flags + FL_EPOCH * 8, RLX_AGENT);
  const tag_t tag = (((tag_t)rfl((unsigned)(e0 >> 32)) << 32) | rfl((unsigned)e0)) + 1ull;
  proj::phase1(Ax, ckv, wq, wuk, wuv, Bx, wqkv, Qf, Kf, Vf, wphi, PhiQ, Part, wo, Bwo, WfA, WfB, flags, tag, DBG(0));
  attn::phase2(Qf, Kf, Vf, Cf, PhiQ, Part, Of, flags, tag, DBG(1));
  outp::phase3(Cf, Of, WfA, WfB, out, Ex, flags, tag, DBG(2));
}
extern "C" void kernel_launch(void* const* d_in, const int* in_sizes, int n_in, void* d_out, int out_size, void* d_ws, size_t ws_size, hipStream_t stream) {
  (void)hipFuncSetAttribute((const void*)fused_kernel, hipFuncAttributeMaxDynamicSharedMemorySize, 163840);
  fused_kernel<<<256, 512, 163840, stream>>>((const u16*)d_in[0], (const u16*)d_in[1], (const u16*)d_in[2], (const u16*)d_in[3], (const u16*)d_in[4], (const u16*)d_in[5], (const u16*)d_in[6],
                                             (const u16*)d_in[7], (const u16*)d_in[8], (const u16*)d_in[9], (u16*)d_out, (char*)d_ws DBGARG);
}
```
